```python
import torch

def _softmax(x, axis=-1):
    return torch.softmax(x, dim=axis)
def _gelu(x):
    return torch.nn.functional.gelu(x, approximate="tanh")
def _silu(x):
    return torch.nn.functional.silu(x)
def _sigmoid(x):
    return torch.sigmoid(x)
def _layernorm(x, axis=-1, eps=1e-5):
    m = x.mean(dim=axis, keepdim=True)
    v = x.var(dim=axis, keepdim=True, unbiased=False)
    return (x - m) / torch.sqrt(v + eps)
def _scatter_add(tgt, idx, src, axis=0):
    return tgt.clone().index_add_(axis, idx, src)
def _segment_sum(src, seg, n):
    out = torch.zeros((n,) + src.shape[1:], dtype=src.dtype)
    return out.index_add_(0, seg, src)
def _pad(x, pw):
    flat = tuple(v for pair in reversed(pw) for v in pair)
    return torch.nn.functional.pad(x, flat)

kernel_name = 'interleave_layers__compressed_kv_cla__swiglu_ffn__decode'

def setup_inputs():

    _w = (lambda a: (lambda t: t.to(torch.bfloat16) if t.is_floating_point() else t)(torch.as_tensor(a)))
    def _A_setup():
        B, H, Sq, Sk, D, M = 4, 32, 1, 4096, 128, 512
        Dc = 4 * D
        return {
            "x": _w((lambda *s: torch.randn(*s, dtype=torch.bfloat16) * 0.05)(B, Sq, M)),
            "c_kv": _w((lambda *s: torch.randn(*s, dtype=torch.bfloat16) * 1.6)(B, Sk, Dc)),
            "w_q": _w((lambda *s: torch.randn(*s, dtype=torch.bfloat16) * 1.6)(M, H, D) * (M ** -0.5)),
            "w_uk": _w((lambda *s: torch.randn(*s, dtype=torch.bfloat16) * 1.6)(Dc, H, D) * (Dc ** -0.5)),
            "w_uv": _w((lambda *s: torch.randn(*s, dtype=torch.bfloat16) * 1.6)(Dc, H, D) * (Dc ** -0.5)),
            "w_o": _w((lambda *s: torch.randn(*s, dtype=torch.bfloat16) * 1.6)(H, D, M) * ((H * D) ** -0.5)),
        }
    _A_in = _A_setup()

    def _B_setup():
        B, S, D, F = 4, 1, 512, 1792
        return {
            "x": _w((lambda *s: torch.randn(*s, dtype=torch.bfloat16) * 0.05)(B, S, D)),
            "w_gate": _w((lambda *s: torch.randn(*s, dtype=torch.bfloat16) * 0.05)(D, F)),
            "w_up": _w((lambda *s: torch.randn(*s, dtype=torch.bfloat16) * 0.05)(D, F)),
            "w_down": _w((lambda *s: torch.randn(*s, dtype=torch.bfloat16) * 0.05)(F, D)),
        }
    _B_in = _B_setup()

    return {
        "A_x": _A_in["x"],
        "A_c_kv": _A_in["c_kv"],
        "A_w_q": _A_in["w_q"],
        "A_w_uk": _A_in["w_uk"],
        "A_w_uv": _A_in["w_uv"],
        "A_w_o": _A_in["w_o"],
        "B_x": _B_in["x"],
        "B_w_gate": _B_in["w_gate"],
        "B_w_up": _B_in["w_up"],
        "B_w_down": _B_in["w_down"],
    }

def reference(A_x, A_c_kv, A_w_q, A_w_uk, A_w_uv, A_w_o, B_x, B_w_gate, B_w_up, B_w_down):
    def _A_ref(x, c_kv, w_q, w_uk, w_uv, w_o):
        B, Sq, M = x.shape[0], x.shape[1], x.shape[2]
        Sk = c_kv.shape[1]
        H, D = w_q.shape[1], w_q.shape[2]
        q = torch.einsum("bqm,mhd->bhqd", x, w_q)
        k = torch.einsum("bkc,chd->bhkd", c_kv, w_uk)
        v = torch.einsum("bkc,chd->bhkd", c_kv, w_uv)
        scores = torch.einsum("bhqd,bhkd->bhqk", q, k) * (D ** -0.5)
        off = Sk - Sq
        qi = torch.arange(Sq)[:, None]
        kj = torch.arange(Sk)[None, :]
        cmask = kj <= (qi + off)
        scores = torch.where(cmask[None, None], scores, -1e9)
        p = _softmax(scores, axis=-1)
        ctx = torch.einsum("bhqk,bhkd->bhqd", p, v)
        return torch.einsum("bhqd,hdm->bqm", ctx, w_o)

    def _B_ref(x, w_gate, w_up, w_down):
        g = _silu(x @ w_gate)
        u = x @ w_up
        return (g * u) @ w_down

    x = A_x
    x = x + _A_ref(_layernorm(x, axis=-1), A_c_kv, A_w_q, A_w_uk, A_w_uv, A_w_o)
    x = x + _B_ref(_layernorm(x, axis=-1), B_w_gate, B_w_up, B_w_down)
    x = x + _A_ref(_layernorm(x, axis=-1), A_c_kv, A_w_q, A_w_uk, A_w_uv, A_w_o)
    x = x + _B_ref(_layernorm(x, axis=-1), B_w_gate, B_w_up, B_w_down)
    return x
```

```cpp
#include <hip/hip_runtime.h>
#include <hip/hip_bf16.h>
typedef unsigned short u16;
__device__ __forceinline__ float b2f(u16 v){ return __builtin_bit_cast(float, (unsigned)v<<16); }
__device__ __forceinline__ u16 f2b(float f){ unsigned u=__builtin_bit_cast(unsigned,f); u += 0x7FFF + ((u>>16)&1); return (u16)(u>>16);}
constexpr int B=4,H=32,SK=4096,D=128,M=512,DC=512,F=1792;

__global__ void k_init(const u16* x, float* xs){ int i=blockIdx.x*256+threadIdx.x; if(i<B*M) xs[i]=b2f(x[i]); }
__device__ void ln_all(const float* xs, float* sh){
  int w=threadIdx.x>>6, l=threadIdx.x&63;
  float v[8]; float s=0; for(int j=0;j<8;j++){ v[j]=xs[w*M+l+64*j]; s+=v[j]; }
  for(int o=32;o;o>>=1) s+=__shfl_xor(s,o);
  float m=s/M; float q=0; for(int j=0;j<8;j++){ float d=v[j]-m; q+=d*d; }
  for(int o=32;o;o>>=1) q+=__shfl_xor(q,o);
  float r=rsqrtf(q/M+1e-5f);
  for(int j=0;j<8;j++) sh[w*M+l+64*j]=(v[j]-m)*r;
  __syncthreads();
}
__global__ void k_q(const float* xs,const u16* wq,const u16* wuk,float* qabs){
  __shared__ float xn[B*M]; __shared__ float q[B*D];
  ln_all(xs,xn);
  int h=blockIdx.x>>3, cs=blockIdx.x&7;
  for(int i=threadIdx.x;i<B*D;i+=256){ int b=i/D,d=i%D; float a=0; for(int m=0;m<M;m++) a+=xn[b*M+m]*b2f(wq[(size_t)m*H*D+h*D+d]); q[i]=a; }
  __syncthreads();
  { int i=threadIdx.x; int b=i/64,c=cs*64+i%64; float a=0; for(int d=0;d<D;d++) a+=q[b*D+d]*b2f(wuk[(size_t)c*H*D+h*D+d]); qabs[(b*H+h)*DC+c]=a*0.08838834764831845f; }
}
__global__ void k_attn(const float* qabs,const u16* ckv,float* part,float* ml){
  __shared__ float s[H*64];
  int b=blockIdx.x>>6, sp=blockIdx.x&63;
  const u16* kv=ckv+((size_t)b*SK+sp*64)*DC;
  for(int i=threadIdx.x;i<H*64;i+=256){ int h=i/64,k=i%64; float a=0; for(int c=0;c<DC;c++) a+=qabs[(b*H+h)*DC+c]*b2f(kv[k*DC+c]); s[i]=a; }
  __syncthreads();
  if(threadIdx.x<H){ int h=threadIdx.x; float m=-1e30f; for(int k=0;k<64;k++) m=fmaxf(m,s[h*64+k]); float l=0; for(int k=0;k<64;k++){ float p=__expf(s[h*64+k]-m); s[h*64+k]=p; l+=p;} ml[((b*64+sp)*H+h)*2]=m; ml[((b*64+sp)*H+h)*2+1]=l; }
  __syncthreads();
  for(int i=threadIdx.x;i<H*DC;i+=256){ int h=i/DC,c=i%DC; float a=0; for(int k=0;k<64;k++) a+=s[h*64+k]*b2f(kv[k*DC+c]); part[((size_t)(b*64+sp)*H+h)*DC+c]=a; }
}
__global__ void k_ctx(const float* part,const float* ml,const u16* wuv,float* ctx){
  __shared__ float cl[DC]; __shared__ float w[64];
  int b=blockIdx.x/H,h=blockIdx.x%H;
  if(threadIdx.x==0){ float m=-1e30f; for(int s=0;s<64;s++) m=fmaxf(m,ml[((b*64+s)*H+h)*2]); float L=0; for(int s=0;s<64;s++){ float e=__expf(ml[((b*64+s)*H+h)*2]-m); w[s]=e; L+=e*ml[((b*64+s)*H+h)*2+1]; } for(int s=0;s<64;s++) w[s]/=L; }
  __syncthreads();
  for(int c=threadIdx.x;c<DC;c+=256){ float a=0; for(int s=0;s<64;s++) a+=w[s]*part[((size_t)(b*64+s)*H+h)*DC+c]; cl[c]=a; }
  __syncthreads();
  if(threadIdx.x<D){ int d=threadIdx.x; float a=0; for(int c=0;c<DC;c++) a+=cl[c]*b2f(wuv[(size_t)c*H*D+h*D+d]); ctx[(b*H+h)*D+d]=a; }
}
__global__ void k_o(const float* ctx,const u16* wo,const float* xin,float* xout){
  __shared__ float r[256];
  int b=blockIdx.x>>3, m=(blockIdx.x&7)*64+(threadIdx.x&63), ks=threadIdx.x>>6;
  float a=0; for(int k=ks*1024;k<ks*1024+1024;k++) a+=ctx[b*H*D+k]*b2f(wo[(size_t)k*M+m]);
  r[threadIdx.x]=a; __syncthreads();
  if(ks==0){ float t=r[threadIdx.x]+r[threadIdx.x+64]+r[threadIdx.x+128]+r[threadIdx.x+192]; xout[b*M+m]=xin[b*M+m]+t; }
}
__global__ void k_up(const float* xs,const u16* wg,const u16* wu,float* hb){
  __shared__ float xn[B*M];
  ln_all(xs,xn);
  int b=threadIdx.x>>6, f=blockIdx.x*64+(threadIdx.x&63);
  float g=0,u=0; for(int m=0;m<M;m++){ float xv=xn[b*M+m]; g+=xv*b2f(wg[(size_t)m*F+f]); u+=xv*b2f(wu[(size_t)m*F+f]); }
  hb[b*F+f]=g/(1.f+__expf(-g))*u;
}
__global__ void k_down(const float* hb,const u16* wd,const float* xin,float* xout,u16* out){
  __shared__ float r[256];
  int b=blockIdx.x>>3, m=(blockIdx.x&7)*64+(threadIdx.x&63), ks=threadIdx.x>>6;
  float a=0; for(int k=ks*448;k<ks*448+448;k++) a+=hb[b*F+k]*b2f(wd[(size_t)k*M+m]);
  r[threadIdx.x]=a; __syncthreads();
  if(ks==0){ float t=r[threadIdx.x]+r[threadIdx.x+64]+r[threadIdx.x+128]+r[threadIdx.x+192]; float v=xin[b*M+m]+t; xout[b*M+m]=v; if(out) out[b*M+m]=f2b(v);}
}
extern "C" void kernel_launch(void* const* d_in, const int* in_sizes, int n_in, void* d_out, int out_size, void* d_ws, size_t ws_size, hipStream_t stream){
  const u16* x=(const u16*)d_in[0]; const u16* ckv=(const u16*)d_in[1]; const u16* wq=(const u16*)d_in[2]; const u16* wuk=(const u16*)d_in[3]; const u16* wuv=(const u16*)d_in[4]; const u16* wo=(const u16*)d_in[5];
  const u16* wg=(const u16*)d_in[7]; const u16* wu=(const u16*)d_in[8]; const u16* wd=(const u16*)d_in[9];
  float* ws=(float*)d_ws; float* xa=ws; float* xb=ws+2048; float* qabs=ws+4096; float* ml=qabs+B*H*DC; float* ctx=ml+B*64*H*2; float* hb=ctx+B*H*D; float* part=hb+B*2048;
  k_init<<<8,256,0,stream>>>(x,xa);
  for(int l=0;l<2;l++){
    k_q<<<256,256,0,stream>>>(xa,wq,wuk,qabs);
    k_attn<<<256,256,0,stream>>>(qabs,ckv,part,ml);
    k_ctx<<<B*H,256,0,stream>>>(part,ml,wuv,ctx);
    k_o<<<32,256,0,stream>>>(ctx,wo,xa,xb);
    k_up<<<F/64,256,0,stream>>>(xb,wg,wu,hb);
    k_down<<<32,256,0,stream>>>(hb,wd,xb,xa,l==1?(u16*)d_out:nullptr);
  }
}
```

```cpp
#define METRIC 0
#include <hip/hip_runtime.h>
#include <hip/hip_bf16.h>
typedef unsigned short u16;
__device__ __forceinline__ float b2f(u16 v){ return __builtin_bit_cast(float, (unsigned)v<<16); }
__device__ __forceinline__ u16 f2b(float f){ unsigned u=__builtin_bit_cast(unsigned,f); u += 0x7FFF + ((u>>16)&1); return (u16)(u>>16);}
constexpr int B=4,H=32,SK=4096,D=128,M=512,DC=512,F=1792;

__global__ void k_init(const u16* x, float* xs){ int i=blockIdx.x*256+threadIdx.x; if(i<B*M) xs[i]=b2f(x[i]); }
__device__ void ln_all(const float* xs, float* sh){
  int w=threadIdx.x>>6, l=threadIdx.x&63;
  float v[8]; float s=0; for(int j=0;j<8;j++){ v[j]=xs[w*M+l+64*j]; s+=v[j]; }
  for(int o=32;o;o>>=1) s+=__shfl_xor(s,o);
  float m=s/M; float q=0; for(int j=0;j<8;j++){ float d=v[j]-m; q+=d*d; }
  for(int o=32;o;o>>=1) q+=__shfl_xor(q,o);
  float r=rsqrtf(q/M+1e-5f);
  for(int j=0;j<8;j++) sh[w*M+l+64*j]=(v[j]-m)*r;
  __syncthreads();
}
__global__ void k_q(const float* xs,const u16* wq,const u16* wuk,float* qabs){
  __shared__ float xn[B*M]; __shared__ float q[B*D];
  ln_all(xs,xn);
  int h=blockIdx.x>>3, cs=blockIdx.x&7;
  for(int i=threadIdx.x;i<B*D;i+=256){ int b=i/D,d=i%D; float a=0; for(int m=0;m<M;m++) a+=xn[b*M+m]*b2f(wq[(size_t)m*H*D+h*D+d]); q[i]=a; }
  __syncthreads();
  { int i=threadIdx.x; int b=i/64,c=cs*64+i%64; float a=0; for(int d=0;d<D;d++) a+=q[b*D+d]*b2f(wuk[(size_t)c*H*D+h*D+d]); qabs[(b*H+h)*DC+c]=a*0.08838834764831845f; }
}
__global__ void k_attn(const float* qabs,const u16* ckv,float* part,float* ml){
  __shared__ float s[H*64];
  int b=blockIdx.x>>6, sp=blockIdx.x&63;
  const u16* kv=ckv+((size_t)b*SK+sp*64)*DC;
  for(int i=threadIdx.x;i<H*64;i+=256){ int h=i/64,k=i%64; float a=0; for(int c=0;c<DC;c++) a+=qabs[(b*H+h)*DC+c]*b2f(kv[k*DC+c]); s[i]=a; }
  __syncthreads();
  if(threadIdx.x<H){ int h=threadIdx.x; float m=-1e30f; for(int k=0;k<64;k++) m=fmaxf(m,s[h*64+k]); float l=0; for(int k=0;k<64;k++){ float p=__expf(s[h*64+k]-m); s[h*64+k]=p; l+=p;} ml[((b*64+sp)*H+h)*2]=m; ml[((b*64+sp)*H+h)*2+1]=l; }
  __syncthreads();
  for(int i=threadIdx.x;i<H*DC;i+=256){ int h=i/DC,c=i%DC; float a=0; for(int k=0;k<64;k++) a+=s[h*64+k]*b2f(kv[k*DC+c]); part[((size_t)(b*64+sp)*H+h)*DC+c]=a; }
}
__global__ void k_ctx(const float* part,const float* ml,const u16* wuv,float* ctx){
  __shared__ float cl[DC]; __shared__ float w[64];
  int b=blockIdx.x/H,h=blockIdx.x%H;
  if(threadIdx.x==0){ float m=-1e30f; for(int s=0;s<64;s++) m=fmaxf(m,ml[((b*64+s)*H+h)*2]); float L=0; for(int s=0;s<64;s++){ float e=__expf(ml[((b*64+s)*H+h)*2]-m); w[s]=e; L+=e*ml[((b*64+s)*H+h)*2+1]; } for(int s=0;s<64;s++) w[s]/=L; }
  __syncthreads();
  for(int c=threadIdx.x;c<DC;c+=256){ float a=0; for(int s=0;s<64;s++) a+=w[s]*part[((size_t)(b*64+s)*H+h)*DC+c]; cl[c]=a; }
  __syncthreads();
  if(threadIdx.x<D){ int d=threadIdx.x; float a=0; for(int c=0;c<DC;c++) a+=cl[c]*b2f(wuv[(size_t)c*H*D+h*D+d]); ctx[(b*H+h)*D+d]=a; }
}
__global__ void k_o(const float* ctx,const u16* wo,const float* xin,float* xout){
  __shared__ float r[256];
  int b=blockIdx.x>>3, m=(blockIdx.x&7)*64+(threadIdx.x&63), ks=threadIdx.x>>6;
  float a=0; for(int k=ks*1024;k<ks*1024+1024;k++) a+=ctx[b*H*D+k]*b2f(wo[(size_t)k*M+m]);
  r[threadIdx.x]=a; __syncthreads();
  if(ks==0){ float t=r[threadIdx.x]+r[threadIdx.x+64]+r[threadIdx.x+128]+r[threadIdx.x+192]; xout[b*M+m]=xin[b*M+m]+t; }
}
__global__ void k_up(const float* xs,const u16* wg,const u16* wu,float* hb){
  __shared__ float xn[B*M];
  ln_all(xs,xn);
  int b=threadIdx.x>>6, f=blockIdx.x*64+(threadIdx.x&63);
  float g=0,u=0; for(int m=0;m<M;m++){ float xv=xn[b*M+m]; g+=xv*b2f(wg[(size_t)m*F+f]); u+=xv*b2f(wu[(size_t)m*F+f]); }
  hb[b*F+f]=g/(1.f+__expf(-g))*u;
}
__global__ void k_down(const float* hb,const u16* wd,const float* xin,float* xout,u16* out){
  __shared__ float r[256];
  int b=blockIdx.x>>3, m=(blockIdx.x&7)*64+(threadIdx.x&63), ks=threadIdx.x>>6;
  float a=0; for(int k=ks*448;k<ks*448+448;k++) a+=hb[b*F+k]*b2f(wd[(size_t)k*M+m]);
  r[threadIdx.x]=a; __syncthreads();
  if(ks==0){ float t=r[threadIdx.x]+r[threadIdx.x+64]+r[threadIdx.x+128]+r[threadIdx.x+192]; float v=xin[b*M+m]+t; xout[b*M+m]=v; if(out) out[b*M+m]=f2b(v);}
}

#define NTP ""
typedef unsigned v4u __attribute__((ext_vector_type(4)));
__device__ __forceinline__ unsigned long long rt() { unsigned long long t; asm volatile("s_memrealtime %0\n\ts_waitcnt lgkmcnt(0)" : "=s"(t) :: "memory"); return t; }
#define LD(ptr) asm volatile("global_load_dwordx4 %0, %1, off " NTP : "+v"(dummy) : "v"(ptr) : "memory")
__global__ void __launch_bounds__(256) k_probe(const u16* ckv, const u16* wq, const u16* wuk, const u16* wuv, const u16* wo, const u16* wg_, const u16* wu, const u16* wd, unsigned long long* st) {
  extern __shared__ unsigned char lds[];
  unsigned long long t0 = rt();
  const int wg = blockIdx.x, tid = threadIdx.x, w = tid >> 6, l = tid & 63;
  const int xcd = wg & 7, r = wg >> 3;
  const int h = xcd * 4 + (r >> 3), cs = r & 7;
  v4u dummy = {0,0,0,0};
  for (int i = 0; i < 4; i++) { const u16* p = wuk + (size_t)(cs * 64 + w * 16 + i * 4 + (l >> 4)) * 4096 + h * 128 + (l & 15) * 8; LD(p); }
  for (int i = 0; i < 32; i++) { const u16* p = wq + (size_t)(w * 128 + i * 4 + (l >> 4)) * 4096 + h * 128 + (l & 15) * 8; LD(p); }
  const int b = xcd >> 1, ks = (xcd & 1) * 16 + (r & 15);
  for (int i = 0; i < 24; i++) { const u16* p = ckv + ((size_t)b * 4096 + ks * 128 + w * 32 + i) * 512 + l * 8; LD(p); }
  asm volatile("s_waitcnt vmcnt(24)" ::: "memory");
  unsigned long long t1 = rt();
  for (int i = 24; i < 32; i++) { const u16* p = ckv + ((size_t)b * 4096 + ks * 128 + w * 32 + i) * 512 + l * 8; LD(p); }
  const int dh = r & 1;
  for (int i = 0; i < 16; i++) { const u16* p = wuv + (size_t)(w * 128 + i * 8 + (l >> 3)) * 4096 + h * 128 + dh * 64 + (l & 7) * 8; LD(p); }
  for (int i = 0; i < 15; i++) { const u16* p = wo + (size_t)(h * 128 + dh * 64 + w * 16 + i) * 512 + l * 8; LD(p); }
  asm volatile("s_waitcnt vmcnt(31)" ::: "memory");
  unsigned long long t2 = rt();
  for (int i = 15; i < 16; i++) { const u16* p = wo + (size_t)(h * 128 + dh * 64 + w * 16 + i) * 512 + l * 8; LD(p); }
  for (int i = 0; i < 2; i++) { int e = i * 256 + tid; if (e >= 448) e = 0; const u16* p = wg_ + (size_t)wg * 3584 + e * 8; LD(p); p = wu + (size_t)wg * 3584 + e * 8; LD(p); p = wd + (size_t)wg * 3584 + e * 8; LD(p); }
  asm volatile("s_waitcnt vmcnt(6)" ::: "memory");
  unsigned long long t3 = rt();
  asm volatile("s_waitcnt vmcnt(0)" : "+v"(dummy) :: "memory");
  unsigned long long t4 = rt();
  if (l == 0) { unsigned long long* o = st + (wg * 4 + w) * 8; o[0] = t0; o[1] = t1; o[2] = t2; o[3] = t3; o[4] = t4; }
}
__global__ void k_amp(const unsigned long long* st) {
  unsigned long long mn = ~0ull, mx = 0;
  for (int i = 0; i < 1024; i++) { unsigned long long a = st[i * 8], b = st[i * 8 + METRIC]; mn = a < mn ? a : mn; mx = b > mx ? b : mx; }
  unsigned long long d = mx - mn; if (d > 5000) d = 5000;
  unsigned long long T = rt() + d * 100;
  for (int i = 0; i < 10000000; i++) if (rt() >= T) break;
}
extern "C" void kernel_launch(void* const* d_in, const int* in_sizes, int n_in, void* d_out, int out_size, void* d_ws, size_t ws_size, hipStream_t stream){
  const u16* x=(const u16*)d_in[0]; const u16* ckv=(const u16*)d_in[1]; const u16* wq=(const u16*)d_in[2]; const u16* wuk=(const u16*)d_in[3]; const u16* wuv=(const u16*)d_in[4]; const u16* wo=(const u16*)d_in[5];
  const u16* wg=(const u16*)d_in[7]; const u16* wu=(const u16*)d_in[8]; const u16* wd=(const u16*)d_in[9];
  float* ws=(float*)d_ws; float* xa=ws; float* xb=ws+2048; float* qabs=ws+4096; float* ml=qabs+B*H*DC; float* ctx=ml+B*64*H*2; float* hb=ctx+B*H*D; float* part=hb+B*2048;
  unsigned long long* st=(unsigned long long*)((char*)d_ws+(128u<<20));
  hipFuncSetAttribute((const void*)k_probe, hipFuncAttributeMaxDynamicSharedMemorySize, 96*1024);
  k_probe<<<256,256,96*1024,stream>>>(ckv,wq,wuk,wuv,wo,wg,wu,wd,st);
  k_amp<<<1,1,0,stream>>>(st);
  k_init<<<8,256,0,stream>>>(x,xa);
  for(int l=0;l<2;l++){
    k_q<<<256,256,0,stream>>>(xa,wq,wuk,qabs);
    k_attn<<<256,256,0,stream>>>(qabs,ckv,part,ml);
    k_ctx<<<B*H,256,0,stream>>>(part,ml,wuv,ctx);
    k_o<<<32,256,0,stream>>>(ctx,wo,xa,xb);
    k_up<<<F/64,256,0,stream>>>(xb,wg,wu,hb);
    k_down<<<32,256,0,stream>>>(hb,wd,xb,xa,l==1?(u16*)d_out:nullptr);
  }
}
```

```cpp
#define KIND 2
#define METRIC 0
#define NTP "nt"
#include <hip/hip_runtime.h>
#include <hip/hip_bf16.h>
typedef unsigned short u16;
__device__ __forceinline__ float b2f(u16 v){ return __builtin_bit_cast(float, (unsigned)v<<16); }
__device__ __forceinline__ u16 f2b(float f){ unsigned u=__builtin_bit_cast(unsigned,f); u += 0x7FFF + ((u>>16)&1); return (u16)(u>>16);}
constexpr int B=4,H=32,SK=4096,D=128,M=512,DC=512,F=1792;

__global__ void k_init(const u16* x, float* xs){ int i=blockIdx.x*256+threadIdx.x; if(i<B*M) xs[i]=b2f(x[i]); }
__device__ void ln_all(const float* xs, float* sh){
  int w=threadIdx.x>>6, l=threadIdx.x&63;
  float v[8]; float s=0; for(int j=0;j<8;j++){ v[j]=xs[w*M+l+64*j]; s+=v[j]; }
  for(int o=32;o;o>>=1) s+=__shfl_xor(s,o);
  float m=s/M; float q=0; for(int j=0;j<8;j++){ float d=v[j]-m; q+=d*d; }
  for(int o=32;o;o>>=1) q+=__shfl_xor(q,o);
  float r=rsqrtf(q/M+1e-5f);
  for(int j=0;j<8;j++) sh[w*M+l+64*j]=(v[j]-m)*r;
  __syncthreads();
}
__global__ void k_q(const float* xs,const u16* wq,const u16* wuk,float* qabs){
  __shared__ float xn[B*M]; __shared__ float q[B*D];
  ln_all(xs,xn);
  int h=blockIdx.x>>3, cs=blockIdx.x&7;
  for(int i=threadIdx.x;i<B*D;i+=256){ int b=i/D,d=i%D; float a=0; for(int m=0;m<M;m++) a+=xn[b*M+m]*b2f(wq[(size_t)m*H*D+h*D+d]); q[i]=a; }
  __syncthreads();
  { int i=threadIdx.x; int b=i/64,c=cs*64+i%64; float a=0; for(int d=0;d<D;d++) a+=q[b*D+d]*b2f(wuk[(size_t)c*H*D+h*D+d]); qabs[(b*H+h)*DC+c]=a*0.08838834764831845f; }
}
__global__ void k_attn(const float* qabs,const u16* ckv,float* part,float* ml){
  __shared__ float s[H*64];
  int b=blockIdx.x>>6, sp=blockIdx.x&63;
  const u16* kv=ckv+((size_t)b*SK+sp*64)*DC;
  for(int i=threadIdx.x;i<H*64;i+=256){ int h=i/64,k=i%64; float a=0; for(int c=0;c<DC;c++) a+=qabs[(b*H+h)*DC+c]*b2f(kv[k*DC+c]); s[i]=a; }
  __syncthreads();
  if(threadIdx.x<H){ int h=threadIdx.x; float m=-1e30f; for(int k=0;k<64;k++) m=fmaxf(m,s[h*64+k]); float l=0; for(int k=0;k<64;k++){ float p=__expf(s[h*64+k]-m); s[h*64+k]=p; l+=p;} ml[((b*64+sp)*H+h)*2]=m; ml[((b*64+sp)*H+h)*2+1]=l; }
  __syncthreads();
  for(int i=threadIdx.x;i<H*DC;i+=256){ int h=i/DC,c=i%DC; float a=0; for(int k=0;k<64;k++) a+=s[h*64+k]*b2f(kv[k*DC+c]); part[((size_t)(b*64+sp)*H+h)*DC+c]=a; }
}
__global__ void k_ctx(const float* part,const float* ml,const u16* wuv,float* ctx){
  __shared__ float cl[DC]; __shared__ float w[64];
  int b=blockIdx.x/H,h=blockIdx.x%H;
  if(threadIdx.x==0){ float m=-1e30f; for(int s=0;s<64;s++) m=fmaxf(m,ml[((b*64+s)*H+h)*2]); float L=0; for(int s=0;s<64;s++){ float e=__expf(ml[((b*64+s)*H+h)*2]-m); w[s]=e; L+=e*ml[((b*64+s)*H+h)*2+1]; } for(int s=0;s<64;s++) w[s]/=L; }
  __syncthreads();
  for(int c=threadIdx.x;c<DC;c+=256){ float a=0; for(int s=0;s<64;s++) a+=w[s]*part[((size_t)(b*64+s)*H+h)*DC+c]; cl[c]=a; }
  __syncthreads();
  if(threadIdx.x<D){ int d=threadIdx.x; float a=0; for(int c=0;c<DC;c++) a+=cl[c]*b2f(wuv[(size_t)c*H*D+h*D+d]); ctx[(b*H+h)*D+d]=a; }
}
__global__ void k_o(const float* ctx,const u16* wo,const float* xin,float* xout){
  __shared__ float r[256];
  int b=blockIdx.x>>3, m=(blockIdx.x&7)*64+(threadIdx.x&63), ks=threadIdx.x>>6;
  float a=0; for(int k=ks*1024;k<ks*1024+1024;k++) a+=ctx[b*H*D+k]*b2f(wo[(size_t)k*M+m]);
  r[threadIdx.x]=a; __syncthreads();
  if(ks==0){ float t=r[threadIdx.x]+r[threadIdx.x+64]+r[threadIdx.x+128]+r[threadIdx.x+192]; xout[b*M+m]=xin[b*M+m]+t; }
}
__global__ void k_up(const float* xs,const u16* wg,const u16* wu,float* hb){
  __shared__ float xn[B*M];
  ln_all(xs,xn);
  int b=threadIdx.x>>6, f=blockIdx.x*64+(threadIdx.x&63);
  float g=0,u=0; for(int m=0;m<M;m++){ float xv=xn[b*M+m]; g+=xv*b2f(wg[(size_t)m*F+f]); u+=xv*b2f(wu[(size_t)m*F+f]); }
  hb[b*F+f]=g/(1.f+__expf(-g))*u;
}
__global__ void k_down(const float* hb,const u16* wd,const float* xin,float* xout,u16* out){
  __shared__ float r[256];
  int b=blockIdx.x>>3, m=(blockIdx.x&7)*64+(threadIdx.x&63), ks=threadIdx.x>>6;
  float a=0; for(int k=ks*448;k<ks*448+448;k++) a+=hb[b*F+k]*b2f(wd[(size_t)k*M+m]);
  r[threadIdx.x]=a; __syncthreads();
  if(ks==0){ float t=r[threadIdx.x]+r[threadIdx.x+64]+r[threadIdx.x+128]+r[threadIdx.x+192]; float v=xin[b*M+m]+t; xout[b*M+m]=v; if(out) out[b*M+m]=f2b(v);}
}

typedef unsigned v4u __attribute__((ext_vector_type(4)));
__device__ __forceinline__ unsigned long long rt() { unsigned long long t; asm volatile("s_memrealtime %0\n\ts_waitcnt lgkmcnt(0)" : "=s"(t) :: "memory"); return t; }
#define LD(ptr) asm volatile("global_load_dwordx4 %0, %1, off " NTP : "+v"(dummy) : "v"(ptr) : "memory")
__global__ void __launch_bounds__(256) k_probe(const u16* ckv, const u16* wq, const u16* wuk, unsigned long long* st) {
  extern __shared__ unsigned char lds[];
  unsigned long long t0 = rt();
  const int wg = blockIdx.x, tid = threadIdx.x;
  v4u dummy = {0,0,0,0};
  for (int i = 0; i < 1; i++) { const u16* p = wq + (size_t)wg * 8192 + (i * 256 + tid) * 8; LD(p); }
  asm volatile("s_waitcnt vmcnt(0)" : "+v"(dummy) :: "memory");
  unsigned long long t4 = rt();
  if ((tid & 63) == 0) { unsigned long long* o = st + (wg * 4 + (tid >> 6)) * 2; o[0] = t0; o[1] = t4; }
}
__global__ void k_amp(const unsigned long long* st) {
  __shared__ unsigned long long smn[256], smx[256];
  unsigned long long mn = ~0ull, mx = 0;
  for (int i = threadIdx.x; i < 1024; i += 256) { unsigned long long a = st[i * 2], b = st[i * 2 + METRIC]; mn = a < mn ? a : mn; mx = b > mx ? b : mx; }
  smn[threadIdx.x] = mn; smx[threadIdx.x] = mx; __syncthreads();
  if (threadIdx.x == 0) {
    for (int i = 0; i < 256; i++) { mn = smn[i] < mn ? smn[i] : mn; mx = smx[i] > mx ? smx[i] : mx; }
    unsigned long long d = mx - mn; if (d > 3000) d = 3000;
    unsigned long long T = rt() + d * 1000;
    for (int i = 0; i < 10000000; i++) if (rt() >= T) break;
  }
}
extern "C" void kernel_launch(void* const* d_in, const int* in_sizes, int n_in, void* d_out, int out_size, void* d_ws, size_t ws_size, hipStream_t stream){
  const u16* x=(const u16*)d_in[0]; const u16* ckv=(const u16*)d_in[1]; const u16* wq=(const u16*)d_in[2]; const u16* wuk=(const u16*)d_in[3]; const u16* wuv=(const u16*)d_in[4]; const u16* wo=(const u16*)d_in[5];
  const u16* wg=(const u16*)d_in[7]; const u16* wu=(const u16*)d_in[8]; const u16* wd=(const u16*)d_in[9];
  float* ws=(float*)d_ws; float* xa=ws; float* xb=ws+2048; float* qabs=ws+4096; float* ml=qabs+B*H*DC; float* ctx=ml+B*64*H*2; float* hb=ctx+B*H*D; float* part=hb+B*2048;
  unsigned long long* st=(unsigned long long*)((char*)d_ws+(128u<<20));
  (void)hipFuncSetAttribute((const void*)k_probe, hipFuncAttributeMaxDynamicSharedMemorySize, 96*1024);
  k_probe<<<256,256,96*1024,stream>>>(ckv,wq,wuk,st);
  k_amp<<<1,256,0,stream>>>(st);
  k_init<<<8,256,0,stream>>>(x,xa);
  for(int l=0;l<2;l++){
    k_q<<<256,256,0,stream>>>(xa,wq,wuk,qabs);
    k_attn<<<256,256,0,stream>>>(qabs,ckv,part,ml);
    k_ctx<<<B*H,256,0,stream>>>(part,ml,wuv,ctx);
    k_o<<<32,256,0,stream>>>(ctx,wo,xa,xb);
    k_up<<<F/64,256,0,stream>>>(xb,wg,wu,hb);
    k_down<<<32,256,0,stream>>>(hb,wd,xb,xa,l==1?(u16*)d_out:nullptr);
  }
}
```

```cpp
#include <hip/hip_runtime.h>
#include <hip/hip_bf16.h>

struct Srd { const void* p; unsigned n; unsigned f; };
struct Args { Srd b[11]; };

__global__ void __launch_bounds__(256, 1) mla_ffn_decode_kernel(Args a) {
  asm volatile(
    ".p2align 12\n"
    "s_mov_b64 s[8:9], %0\n"
    "s_mov_b32 s56, %1\n"
    "s_load_dwordx16 s[12:27], s[8:9], 0x0\n"
    "s_load_dwordx16 s[28:43], s[8:9], 0x40\n"
    "s_load_dwordx8 s[44:51], s[8:9], 0x80\n"
    "s_load_dwordx4 s[52:55], s[8:9], 0xa0\n"
    "s_getpc_b64 s[80:81]\n"
    "Lpc:\n"
    "s_and_b32 s84, s56, 16\n"
    "s_lshl_b32 s84, s84, 11\n"
    "v_lshl_add_u32 v144, v0, 7, s84\n"
    "v_min_u32 v144, Ltret-Lpc, v144\n"
    "s_cmp_ge_u32 s56, 24\n"
    "s_cbranch_scc1 Lnopf\n"
    "global_load_dword v231, v144, s[80:81]\n"
    "Lnopf:\n"
    "v_and_b32 v1, 63, v0\n"
    "v_lshrrev_b32 v145, 6, v0\n"
    "s_nop 0\n"
    "v_readfirstlane_b32 s59, v145\n"
    "v_and_b32 v2, 15, v0\n"
    "v_bfe_u32 v3, v0, 4, 2\n"
    "v_bfe_u32 v9, v0, 2, 2\n"
    "v_and_b32 v10, 3, v0\n"
    "s_and_b32 s57, s56, 7\n"
    "s_lshr_b32 s58, s56, 3\n"
    "s_and_b32 s64, s58, 1\n"
    "s_lshl_b32 s60, s57, 2\n"
    "s_bfe_u32 s85, s58, 0x10004\n"
    "s_lshl_b32 s86, s85, 1\n"
    "s_add_u32 s60, s60, s86\n"
    "s_add_u32 s60, s60, s64\n"
    "s_bfe_u32 s61, s58, 0x30001\n"
    "s_and_b32 s69, s60, 15\n"
    "s_bfe_u32 s68, s57, 0x10002\n"
    "s_lshl_b32 s68, s68, 13\n"
    "s_bfe_u32 s85, s69, 0x30001\n"
    "s_lshl_b32 s84, s85, 14\n"
    "s_mul_i32 s86, s64, 12288\n"
    "s_add_u32 s84, s84, s86\n"
    "s_bfe_u32 s85, s61, 0x10000\n"
    "s_lshl_b32 s86, s85, 11\n"
    "s_add_u32 s84, s84, s86\n"
    "s_bfe_u32 s85, s61, 0x20001\n"
    "s_lshl_b32 s86, s85, 8\n"
    "s_add_u32 s84, s84, s86\n"
    "s_xor_b32 s84, s84, s68\n"
    "v_lshrrev_b32 v151, 5, v1\n"
    "s_add_u32 s87, s84, 0xf0000\n"
    "v_lshl_add_u32 v150, v3, 10, s87\n"
    "v_lshl_add_u32 v150, v2, 4, v150\n"
    "v_lshl_add_u32 v150, v151, 31, v150\n"
    "s_lshl_b32 s84, s64, 15\n"
    "s_bfe_u32 s85, s60, 0x10004\n"
    "s_lshl_b32 s86, s85, 14\n"
    "s_add_u32 s84, s84, s86\n"
    "s_bfe_u32 s85, s60, 0x30001\n"
    "s_lshl_b32 s86, s85, 10\n"
    "s_add_u32 s84, s84, s86\n"
    "s_lshl_b32 s86, s61, 7\n"
    "s_add_u32 s84, s84, s86\n"
    "v_bfe_u32 v151, v0, 3, 1\n"
    "v_and_b32 v152, 7, v0\n"
    "s_add_u32 s87, s84, 0x110000\n"
    "v_lshl_add_u32 v148, v151, 16, s87\n"
    "v_lshl_add_u32 v148, v3, 13, v148\n"
    "v_lshl_add_u32 v148, v152, 4, v148\n"
    "s_lshl_b32 s84, s56, 9\n"
    "s_add_u32 s87, s84, 0x12fe00\n"
    "v_lshl_add_u32 v151, v1, 4, s87\n"
    "v_cmp_gt_u32 vcc, 32, v1\n"
    "s_nop 1\n"
    "v_cndmask_b32 v148, v151, v148, vcc\n"
    "v_mov_b32 v84, -1\n"
    "v_mov_b32 v85, -1\n"
    "v_mov_b32 v86, -1\n"
    "v_mov_b32 v87, -1\n"
    "v_mov_b32 v98, 0x200000\n"
    "s_lshl_b32 s84, s61, 19\n"
    "s_lshl_b32 s86, s59, 17\n"
    "s_add_u32 s84, s84, s86\n"
    "s_lshl_b32 s86, s60, 8\n"
    "s_add_u32 s84, s84, s86\n"
    "v_lshlrev_b32 v159, 2, v3\n"
    "v_xor_b32 v159, v159, v2\n"
    "v_lshl_add_u32 v82, v3, 13, s84\n"
    "v_lshl_add_u32 v82, v159, 4, v82\n"
    "s_lshl_b32 s84, s59, 8\n"
    "v_lshl_add_u32 v242, v10, 10, s84\n"
    "v_lshl_add_u32 v242, v3, 6, v242\n"
    "v_lshl_add_u32 v242, v9, 4, v242\n"
    "v_lshl_add_u32 v158, v9, 10, 0\n"
    "v_lshl_add_u32 v158, v3, 6, v158\n"
    "v_lshl_add_u32 v158, v10, 4, v158\n"
    "v_lshrrev_b32 v153, 3, v1\n"
    "v_bfe_u32 v154, v0, 3, 2\n"
    "v_lshlrev_b32 v154, 1, v154\n"
    "v_xor_b32 v154, v154, v152\n"
    "s_bfe_u32 s85, s61, 0x20001\n"
    "s_lshl_b32 s84, s85, 20\n"
    "s_lshl_b32 s86, s60, 8\n"
    "s_add_u32 s84, s84, s86\n"
    "s_bfe_u32 s85, s61, 0x10000\n"
    "s_lshl_b32 s86, s85, 7\n"
    "s_add_u32 s84, s84, s86\n"
    "s_bfe_u32 s85, s59, 0x10000\n"
    "s_lshl_b32 s86, s85, 16\n"
    "s_add_u32 s84, s84, s86\n"
    "v_lshl_add_u32 v155, v153, 13, s84\n"
    "v_lshl_add_u32 v155, v154, 4, v155\n"
    "v_mov_b32 v239, 0x80000000\n"
    "s_lshr_b32 s62, s57, 1\n"
    "s_bfe_u32 s85, s57, 0x10000\n"
    "s_lshl_b32 s63, s85, 4\n"
    "s_bfe_u32 s85, s58, 0x40001\n"
    "s_add_u32 s63, s63, s85\n"
    "s_lshl_b32 s84, s62, 22\n"
    "v_lshl_add_u32 v83, v1, 4, s84\n"
    "s_mov_b32 s82, 1000000\n"
    "s_mov_b32 s10, 1000000\n"
    "s_lshl_b32 s84, s59, 17\n"
    "v_subrev_u32 v156, s84, v82\n"
    "s_bfe_u32 s6, s59, 0x10000\n"
    "s_lshl_b32 s84, s6, 15\n"
    "v_add_u32 v156, s84, v156\n"
    "s_lshl_b32 s84, s6, 10\n"
    "v_add_u32 v83, s84, v83\n"
    "s_mul_i32 s6, s6, 1056\n"
    "v_mov_b32 v157, 156672\n"
    "ds_write_b128 v157, v[84:87] offset:16\n"
    "s_waitcnt lgkmcnt(0)\n"
    "s_memrealtime s[2:3]\n"
    "s_add_u32 s12, s12, 0x1fffff\n"
    "s_addc_u32 s13, s13, 0\n"
    "s_and_b32 s12, s12, 0xffe00000\n"
    "s_cmp_ge_u32 s59, 2\n"
    "s_cbranch_scc1 Lld\n"
    "s_cmp_eq_u32 s59, 0\n"
    "s_cbranch_scc1 LwA\n"
    "s_memtime s[80:81]\n"
    "buffer_store_dwordx4 v[84:87], v150, s[12:15], 0 offen sc1\n"
    "buffer_store_dwordx4 v[84:87], v148, s[12:15], 0 offen sc1\n"
    "buffer_load_dwordx2 v[100:101], v98, s[12:15], 0 offen sc1\n"
    "s_barrier\n"
    "s_lshl_b32 s84, s60, 10\n"
    "s_lshl_b32 s86, s61, 7\n"
    "s_add_u32 s84, s84, s86\n"
    "s_add_u32 s87, s84, 0x204000\n"
    "v_lshl_add_u32 v151, v1, 4, s87\n"
    "s_lshl_b32 s84, s60, 7\n"
    "s_cmp_eq_u32 s61, 0\n"
    "s_cselect_b32 s84, s84, 0x80000000\n"
    "s_add_u32 s87, s84, 0x201000\n"
    "v_lshl_add_u32 v149, v1, 4, s87\n"
    "v_cmp_gt_u32 vcc, 8, v1\n"
    "s_nop 1\n"
    "v_cndmask_b32 v151, v239, v151, vcc\n"
    "v_cndmask_b32 v149, v239, v149, vcc\n"
    "v_lshrrev_b32 v152, 3, v1\n"
    "s_lshl_b32 s84, s60, 10\n"
    "s_add_u32 s87, s84, 0x204000\n"
    "v_lshl_add_u32 v108, v152, 7, s87\n"
    "s_waitcnt vmcnt(2)\n"
    "s_memtime s[78:79]\n"
    "s_waitcnt lgkmcnt(0)\n"
    "s_sub_u32 s84, s78, s80\n"
    "v_mov_b32 v102, s84\n"
    "v_mov_b32 v103, s2\n"
    "s_waitcnt vmcnt(0)\n"
    "v_add_u32 v100, 1, v100\n"
    "buffer_store_dwordx4 v[100:103], v151, s[12:15], 0 offen sc1\n"
    "s_waitcnt lgkmcnt(0)\n"
    "s_mov_b32 s11, s2\n"
    "s_mul_i32 s98, s59, 4224\n"
    "s_mul_i32 s99, s59, 16896\n"
    "s_and_b32 s65, s63, 1\n"
    "v_xor_b32 v228, s59, v9\n"
    "v_xor_b32 v229, s59, v10\n"
    "s_mov_b32 s87, 0x21000\n"
    "v_mul_u32_u24 v230, 4224, v3\n"
    "v_add_u32 v99, s87, v230\n"
    "v_lshl_add_u32 v99, v9, 7, v99\n"
    "v_lshl_add_u32 v99, v10, 3, v99\n"
    "v_lshl_add_u32 v99, v228, 5, v99\n"
    "s_mov_b32 s87, 0x1ce00\n"
    "v_mul_u32_u24 v230, 1056, v9\n"
    "v_add_u32 v104, s87, v230\n"
    "v_lshl_add_u32 v104, v10, 8, v104\n"
    "v_lshl_add_u32 v104, v3, 4, v104\n"
    "v_lshl_add_u32 v104, v229, 6, v104\n"
    "s_lshl_b32 s84, s59, 10\n"
    "s_add_u32 s87, s84, 0x25200\n"
    "v_lshl_add_u32 v127, v1, 4, s87\n"
    "s_add_u32 s84, s84, s68\n"
    "v_lshl_add_u32 v122, v1, 4, s84\n"
    "v_lshl_add_u32 v228, v3, 6, 0\n"
    "v_lshl_add_u32 v228, v10, 4, v228\n"
    "v_lshl_add_u32 v229, v9, 8, 0\n"
    "v_lshl_add_u32 v229, v228, 0, v229\n"
    "s_bfe_u32 s85, s69, 0x30001\n"
    "s_lshl_b32 s4, s85, 14\n"
    "s_lshl_b32 s86, s64, 12\n"
    "s_add_u32 s4, s4, s86\n"
    "s_add_u32 s4, s4, s68\n"
    "s_lshl_b32 s84, s59, 10\n"
    "s_add_u32 s84, s84, s4\n"
    "s_add_u32 s87, s84, 0x40000\n"
    "v_lshl_add_u32 v113, v229, 0, s87\n"
    "v_lshrrev_b32 v226, 1, v3\n"
    "v_and_b32 v227, 1, v3\n"
    "s_bfe_u32 s85, s61, 0x10000\n"
    "s_lshl_b32 s84, s85, 11\n"
    "s_bfe_u32 s85, s59, 0x10001\n"
    "s_lshl_b32 s86, s85, 10\n"
    "s_add_u32 s84, s84, s86\n"
    "s_bfe_u32 s85, s61, 0x20001\n"
    "s_lshl_b32 s86, s85, 8\n"
    "s_add_u32 s84, s84, s86\n"
    "s_bfe_u32 s85, s59, 0x10000\n"
    "s_lshl_b32 s86, s85, 7\n"
    "s_add_u32 s84, s84, s86\n"
    "s_add_u32 s84, s84, s4\n"
    "s_add_u32 s87, s84, 0x40000\n"
    "v_lshl_add_u32 v112, v226, 6, s87\n"
    "v_lshl_add_u32 v112, v10, 4, v112\n"
    "v_lshl_add_u32 v112, v227, 3, v112\n"
    "s_lshl_b32 s84, s59, 10\n"
    "s_lshl_b32 s86, s61, 7\n"
    "s_add_u32 s84, s84, s86\n"
    "s_add_u32 s84, s84, s4\n"
    "v_lshl_add_u32 v123, v228, 0, s84\n"
    "v_cmp_eq_u32 vcc, 0, v9\n"
    "s_nop 1\n"
    "v_cndmask_b32 v112, v239, v112, vcc\n"
    "v_cndmask_b32 v123, v239, v123, vcc\n"
    "s_bfe_u32 s85, s57, 0x20000\n"
    "s_lshl_b32 s84, s85, 15\n"
    "s_bfe_u32 s85, s59, 0x10001\n"
    "s_lshl_b32 s86, s85, 14\n"
    "s_add_u32 s84, s84, s86\n"
    "s_bfe_u32 s85, s59, 0x10000\n"
    "s_lshl_b32 s86, s85, 12\n"
    "s_add_u32 s84, s84, s86\n"
    "s_add_u32 s84, s84, s68\n"
    "v_lshl_add_u32 v114, v229, 0, s84\n"
    "s_bfe_u32 s85, s59, 0x10000\n"
    "s_lshl_b32 s84, s85, 16\n"
    "s_lshl_b32 s86, s64, 15\n"
    "s_add_u32 s84, s84, s86\n"
    "s_bfe_u32 s85, s60, 0x10004\n"
    "s_lshl_b32 s86, s85, 14\n"
    "s_add_u32 s84, s84, s86\n"
    "s_bfe_u32 s85, s60, 0x30001\n"
    "s_lshl_b32 s86, s85, 10\n"
    "s_add_u32 s84, s84, s86\n"
    "s_lshl_b32 s86, s61, 7\n"
    "s_add_u32 s84, s84, s86\n"
    "s_bfe_u32 s85, s59, 0x10001\n"
    "s_lshl_b32 s86, s85, 13\n"
    "s_add_u32 s84, s84, s86\n"
    "s_add_u32 s87, s84, 0x60000\n"
    "v_lshl_add_u32 v115, v1, 1, s87\n"
    "v_lshl_or_b32 v115, v1, 31, v115\n"
    "s_bfe_u32 s4, s60, 0x20003\n"
    "s_xor_b32 s4, s4, s59\n"
    "s_lshl_b32 s84, s69, 18\n"
    "s_lshl_b32 s86, s61, 15\n"
    "s_add_u32 s84, s84, s86\n"
    "s_bfe_u32 s85, s4, 0x10001\n"
    "s_lshl_b32 s86, s85, 14\n"
    "s_add_u32 s84, s84, s86\n"
    "s_bfe_u32 s85, s4, 0x10000\n"
    "s_lshl_b32 s86, s85, 12\n"
    "s_add_u32 s84, s84, s86\n"
    "s_bfe_u32 s85, s60, 0x10001\n"
    "s_lshl_b32 s4, s85, 5\n"
    "s_bfe_u32 s85, s69, 0x10003\n"
    "s_lshl_b32 s85, s85, 13\n"
    "s_xor_b32 s85, s85, s68\n"
    "s_add_u32 s84, s84, s85\n"
    "v_lshlrev_b32 v224, 1, v3\n"
    "v_xor_b32 v224, v224, v1\n"
    "v_xor_b32 v224, s4, v224\n"
    "s_add_u32 s87, s84, 0x400000\n"
    "v_lshl_add_u32 v118, v224, 4, s87\n"
    "s_lshl_b32 s84, s59, 6\n"
    "v_and_b32 v225, 1, v3\n"
    "v_lshrrev_b32 v226, 1, v9\n"
    "v_and_b32 v227, 7, v2\n"
    "s_add_u32 s87, s84, 0x26300\n"
    "v_lshl_add_u32 v135, v9, 4, s87\n"
    "v_lshl_add_u32 v135, v225, 3, v135\n"
    "v_lshl_add_u32 v135, v10, 1, v135\n"
    "s_add_u32 s87, s84, 0x26300\n"
    "v_lshl_add_u32 v136, v3, 4, s87\n"
    "s_mov_b32 s87, 0x21000\n"
    "v_mul_u32_u24 v230, 1056, v2\n"
    "v_add_u32 v124, s87, v230\n"
    "v_lshl_add_u32 v124, v3, 4, v124\n"
    "s_lshl_b32 s84, s59, 2\n"
    "s_add_u32 s87, s84, 0x26200\n"
    "v_lshl_add_u32 v125, v2, 4, s87\n"
    "s_mov_b32 s87, 0x26200\n"
    "v_lshl_add_u32 v126, v2, 4, s87\n"
    "s_add_u32 s84, s59, 1\n"
    "s_and_b32 s84, s84, 3\n"
    "s_lshl_b32 s84, s84, 10\n"
    "s_add_u32 s87, s84, 0x25200\n"
    "v_lshl_add_u32 v128, v1, 4, s87\n"
    "s_add_u32 s84, s59, 2\n"
    "s_and_b32 s84, s84, 3\n"
    "s_lshl_b32 s84, s84, 10\n"
    "s_add_u32 s87, s84, 0x25200\n"
    "v_lshl_add_u32 v129, v1, 4, s87\n"
    "s_add_u32 s84, s59, 3\n"
    "s_and_b32 s84, s84, 3\n"
    "s_lshl_b32 s84, s84, 10\n"
    "s_add_u32 s87, s84, 0x25200\n"
    "v_lshl_add_u32 v130, v1, 4, s87\n"
    "s_lshl_b32 s84, s59, 13\n"
    "s_add_u32 s87, s84, 0x10800\n"
    "v_lshl_add_u32 v131, v3, 9, s87\n"
    "v_lshl_add_u32 v131, v9, 7, v131\n"
    "v_lshl_add_u32 v131, v225, 6, v131\n"
    "v_lshl_add_u32 v131, v226, 5, v131\n"
    "v_lshl_add_u32 v131, v10, 3, v131\n"
    "v_xor_b32 v132, 32, v131\n"
    "v_xor_b32 v133, 64, v131\n"
    "v_xor_b32 v134, 96, v131\n"
    "s_lshl_b32 s84, s59, 4\n"
    "s_add_u32 s87, s84, 0x26a00\n"
    "v_lshl_add_u32 v137, v3, 6, s87\n"
    "s_add_u32 s87, s84, 0x26440\n"
    "v_mov_b32 v143, s87\n"
    "s_mov_b32 s87, 0x26a00\n"
    "v_lshl_add_u32 v138, v3, 6, s87\n"
    "v_lshl_add_u32 v138, v10, 4, v138\n"
    "s_lshl_b32 s84, s59, 8\n"
    "s_add_u32 s87, s84, 0x25200\n"
    "v_lshl_add_u32 v141, v1, 2, s87\n"
    "s_lshl_b32 s84, s59, 10\n"
    "s_add_u32 s87, s84, 0x25200\n"
    "v_lshl_add_u32 v139, v1, 2, s87\n"
    "s_mov_b32 s87, 0x25200\n"
    "v_lshl_add_u32 v140, v1, 2, s87\n"
    "s_lshl_b32 s84, s59, 2\n"
    "v_lshrrev_b32 v224, 1, v3\n"
    "s_add_u32 s87, s84, 0x26440\n"
    "v_lshl_add_u32 v142, v224, 6, s87\n"
    "v_lshl_add_u32 v142, v10, 4, v142\n"
    "v_and_b32 v224, 1, v1\n"
    "s_barrier\n"
    "v_lshl_add_u32 v121, v226, 6, 0\n"
    "v_lshl_add_u32 v121, v3, 4, v121\n"
    "v_lshl_add_u32 v121, v227, 1, v121\n"
    "v_mul_u32_u24 v230, 8190, v224\n"
    "v_add_u32 v121, v121, v230\n"
    "s_bfe_u32 s85, s58, 0x30002\n"
    "s_lshl_b32 s97, s85, 1\n"
    "s_add_u32 s97, s97, s64\n"
    "s_bfe_u32 s85, s62, 0x10000\n"
    "s_lshl_b32 s84, s85, 16\n"
    "s_lshl_b32 s86, s64, 15\n"
    "s_add_u32 s84, s84, s86\n"
    "s_bfe_u32 s85, s59, 0x10001\n"
    "s_lshl_b32 s86, s85, 14\n"
    "s_add_u32 s84, s84, s86\n"
    "s_bfe_u32 s85, s59, 0x10000\n"
    "s_lshl_b32 s86, s85, 12\n"
    "s_add_u32 s84, s84, s86\n"
    "s_add_u32 s84, s84, s68\n"
    "s_add_u32 s87, s84, 0x60000\n"
    "v_lshl_add_u32 v116, v1, 4, s87\n"
    "s_lshl_b32 s84, s64, 18\n"
    "s_lshl_b32 s86, s59, 16\n"
    "s_add_u32 s84, s84, s86\n"
    "v_lshrrev_b32 v224, 1, v3\n"
    "v_xor_b32 v224, v224, v225\n"
    "v_xor_b32 v228, s62, v3\n"
    "v_and_b32 v229, 1, v228\n"
    "v_lshrrev_b32 v228, 1, v228\n"
    "s_add_u32 s87, s84, 0x400000\n"
    "v_lshl_add_u32 v117, v225, 21, s87\n"
    "v_lshl_add_u32 v117, v224, 13, v117\n"
    "v_lshl_add_u32 v117, v2, 3, v117\n"
    "v_lshl_add_u32 v117, v229, 12, v117\n"
    "v_lshl_add_u32 v117, v228, 14, v117\n"
    "s_lshl_b32 s4, s59, 3\n"
    "s_bfe_u32 s85, s63, 0x40001\n"
    "s_xor_b32 s4, s4, s85\n"
    "s_lshl_b32 s84, s65, 20\n"
    "s_lshl_b32 s86, s4, 7\n"
    "s_add_u32 s84, s84, s86\n"
    "s_xor_b32 s70, s84, 0x0\n"
    "s_xor_b32 s71, s84, 0x80200\n"
    "s_xor_b32 s72, s84, 0x100800\n"
    "s_xor_b32 s73, s84, 0x180a00\n"
    "s_xor_b32 s74, s84, 0x8000\n"
    "s_xor_b32 s75, s84, 0x88200\n"
    "s_xor_b32 s76, s84, 0x108800\n"
    "s_xor_b32 s77, s84, 0x188a00\n"
    "s_lshl_b32 s84, s62, 14\n"
    "s_lshl_b32 s86, s64, 12\n"
    "s_add_u32 s84, s84, s86\n"
    "s_lshl_b32 s86, s63, 7\n"
    "s_add_u32 s84, s84, s86\n"
    "s_cmp_eq_u32 s59, 0\n"
    "s_cselect_b32 s84, s84, 0x80000000\n"
    "s_lshl_b32 s85, s65, 1\n"
    "v_xor_b32 v224, s85, v2\n"
    "s_add_u32 s87, s84, 0x80000\n"
    "v_lshl_add_u32 v119, v224, 3, s87\n"
    "v_lshl_add_u32 v119, v3, 13, v119\n"
    "v_cmp_gt_u32 vcc, 2, v3\n"
    "s_nop 1\n"
    "v_cndmask_b32 v119, v239, v119, vcc\n"
    "s_bfe_u32 s4, s60, 0x10002\n"
    "s_lshl_b32 s84, s59, 14\n"
    "s_add_u32 s84, s84, s68\n"
    "s_lshl_b32 s86, s64, 12\n"
    "s_add_u32 s84, s84, s86\n"
    "s_bfe_u32 s85, s60, 0x40001\n"
    "s_barrier\n"
    "buffer_load_dwordx4 v[152:155], v108, s[12:15], 0 offen sc1\n"
    "s_lshl_b32 s86, s85, 3\n"
    "s_add_u32 s84, s84, s86\n"
    "v_xor_b32 v224, s4, v225\n"
    "s_add_u32 s87, s84, 0x80000\n"
    "v_lshl_add_u32 v120, v2, 8, s87\n"
    "v_lshl_add_u32 v120, v224, 7, v120\n"
    "s_mov_b32 s83, 0xb0000\n"
    "s_mul_i32 s84, s56, 1536\n"
    "s_add_u32 s87, s84, 0x90000\n"
    "v_lshl_add_u32 v80, v0, 4, s87\n"
    "v_cmp_gt_u32 vcc, 96, v0\n"
    "s_nop 1\n"
    "v_cndmask_b32 v80, v239, v80, vcc\n"
    "v_lshrrev_b32 v228, 1, v1\n"
    "v_and_b32 v229, 1, v1\n"
    "s_lshl_b32 s84, s58, 7\n"
    "s_lshl_b32 s86, s57, 2\n"
    "s_add_u32 s84, s84, s86\n"
    "s_add_u32 s87, s84, 0x140000\n"
    "v_lshl_add_u32 v88, v0, 13, s87\n"
    "v_cmp_gt_u32 vcc, 2, v0\n"
    "s_nop 1\n"
    "v_cndmask_b32 v88, v239, v88, vcc\n"
    "s_add_u32 s87, s68, 0x140000\n"
    "v_lshl_add_u32 v236, v228, 7, s87\n"
    "v_lshl_add_u32 v236, v229, 4, v236\n"
    "v_mul_u32_u24 v230, 1056, v2\n"
    "v_add_u32 v89, s99, v230\n"
    "v_lshl_add_u32 v89, v3, 4, v89\n"
    "v_mul_u32_u24 v230, 4224, v3\n"
    "v_add_u32 v229, 0, v230\n"
    "v_mul_u32_u24 v230, 1056, v9\n"
    "v_add_u32 v229, v229, v230\n"
    "v_lshl_add_u32 v229, v10, 3, v229\n"
    "s_add_u32 s84, s59, 0\n"
    "s_and_b32 s84, s84, 3\n"
    "s_mul_i32 s84, s84, 16896\n"
    "s_lshl_b32 s86, s59, 8\n"
    "s_add_u32 s84, s84, s86\n"
    "v_add_u32 v90, s84, v229\n"
    "s_add_u32 s84, s59, 1\n"
    "s_and_b32 s84, s84, 3\n"
    "s_mul_i32 s84, s84, 16896\n"
    "s_lshl_b32 s86, s59, 8\n"
    "s_add_u32 s84, s84, s86\n"
    "v_add_u32 v91, s84, v229\n"
    "s_add_u32 s84, s59, 2\n"
    "s_and_b32 s84, s84, 3\n"
    "s_mul_i32 s84, s84, 16896\n"
    "s_lshl_b32 s86, s59, 8\n"
    "s_add_u32 s84, s84, s86\n"
    "v_add_u32 v92, s84, v229\n"
    "s_add_u32 s84, s59, 3\n"
    "s_and_b32 s84, s84, 3\n"
    "s_mul_i32 s84, s84, 16896\n"
    "s_lshl_b32 s86, s59, 8\n"
    "s_add_u32 s84, s84, s86\n"
    "v_add_u32 v93, s84, v229\n"
    "v_lshrrev_b32 v228, 1, v0\n"
    "s_lshl_b32 s84, s57, 19\n"
    "s_lshl_b32 s86, s97, 15\n"
    "s_add_u32 s84, s84, s86\n"
    "v_lshl_add_u32 v244, v228, 7, s84\n"
    "v_add_u32 v245, 0x4000, v244\n"
    "s_mul_i32 s84, s57, 7\n"
    "s_lshr_b32 s84, s84, 1\n"
    "s_lshl_b32 s84, s84, 7\n"
    "s_mul_i32 s86, s97, 114688\n"
    "s_add_u32 s84, s84, s86\n"
    "v_lshrrev_b32 v229, 3, v0\n"
    "v_and_b32 v227, 3, v228\n"
    "v_mul_u32_u24 v230, 3584, v229\n"
    "v_add_u32 v246, s84, v230\n"
    "v_lshl_add_u32 v246, v227, 7, v246\n"
    "v_min_u32 v228, 111, v228\n"
    "s_lshl_b32 s84, s57, 7\n"
    "s_mul_i32 s86, s97, 114688\n"
    "s_add_u32 s84, s84, s86\n"
    "v_lshl_add_u32 v247, v228, 10, s84\n"
    "s_lshl_b32 s84, s57, 19\n"
    "s_lshl_b32 s86, s97, 15\n"
    "s_add_u32 s84, s84, s86\n"
    "s_lshl_b32 s86, s59, 12\n"
    "s_add_u32 s84, s84, s86\n"
    "v_lshl_add_u32 v228, v1, 4, s84\n"
    "v_mov_b32 v229, s61\n"
    "v_and_b32 v229, 1, v229\n"
    "v_cmp_eq_u32 vcc, 0, v229\n"
    "s_nop 1\n"
    "v_cndmask_b32 v82, v228, v82, vcc\n"
    "s_barrier\n"
    "ds_read_b64_tr_b16 v[16:17], v99 offset:0\n"
    "ds_read_b64_tr_b16 v[18:19], v99 offset:512\n"
    "ds_read_b64_tr_b16 v[20:21], v99 offset:1056\n"
    "ds_read_b64_tr_b16 v[22:23], v99 offset:1568\n"
    "ds_read_b64_tr_b16 v[24:25], v99 offset:2112\n"
    "ds_read_b64_tr_b16 v[26:27], v99 offset:2624\n"
    "ds_read_b64_tr_b16 v[28:29], v99 offset:3168\n"
    "ds_read_b64_tr_b16 v[30:31], v99 offset:3680\n"
    "ds_read_b128 v[32:35], v104 offset:0\n"
    "ds_read_b128 v[36:39], v104 offset:4224\n"
    "ds_read_b128 v[40:43], v104 offset:8448\n"
    "ds_read_b128 v[44:47], v104 offset:12672\n"
    "s_waitcnt lgkmcnt(0)\n"
    "s_barrier\n"
    "Lgfc_1:\n"
    "s_waitcnt vmcnt(0)\n"
    "v_cmp_ne_u64 vcc, v[100:101], v[152:153]\n"
    "s_cbranch_vccnz Lgfs_2\n"
    "Lgfd_3:\n"
    "ds_write_b32 v157, v1 offset:16\n"
    "v_min_u32_dpp v154, v154, v154 row_ror:8 row_mask:0xf bank_mask:0xf\n"
    "v_mov_b32 v156, v154\n"
    "s_nop 1\n"
    "v_permlane16_swap_b32 v154, v156\n"
    "v_min_u32 v154, v154, v156\n"
    "v_mov_b32 v156, v154\n"
    "s_nop 1\n"
    "v_permlane32_swap_b32 v154, v156\n"
    "v_min_u32 v154, v154, v156\n"
    "v_subrev_u32 v155, s11, v155\n"
    "s_nop 1\n"
    "v_max_i32_dpp v155, v155, v155 row_ror:8 row_mask:0xf bank_mask:0xf\n"
    "v_mov_b32 v156, v155\n"
    "s_nop 1\n"
    "v_permlane16_swap_b32 v155, v156\n"
    "v_max_i32 v155, v155, v156\n"
    "v_mov_b32 v156, v155\n"
    "s_nop 1\n"
    "v_permlane32_swap_b32 v155, v156\n"
    "v_max_i32 v155, v155, v156\n"
    "v_add_u32 v155, s11, v155\n"
    "v_mov_b32 v152, v100\n"
    "v_mov_b32 v153, v101\n"
    "buffer_store_dwordx4 v[152:155], v149, s[12:15], 0 offen sc1\n"
    "v_and_b32 v156, 31, v1\n"
    "s_mov_b32 s87, 0x201000\n"
    "v_lshl_add_u32 v107, v156, 7, s87\n"
    "s_mov_b64 exec, 0\n"
    "s_call_b64 s[0:1], Lseg_B0_0\n"
    "s_mov_b64 exec, -1\n"
    "s_bitcmp1_b32 s61, 0\n"
    "s_cbranch_scc1 Ltf\n"
    "s_mov_b64 exec, 0\n"
    "s_call_b64 s[0:1], Lseg_C0_0\n"
    "s_mov_b64 exec, -1\n"
    "Ltf:\n"
    "s_waitcnt lgkmcnt(0)\n"
    "Lwt_4:\n"
    "s_memrealtime s[78:79]\n"
    "s_waitcnt lgkmcnt(0)\n"
    "s_sub_u32 s78, s78, s11\n"
    "s_cmp_lt_u32 s78, 330\n"
    "s_cbranch_scc1 Lwt_4\n"
    "buffer_load_dwordx4 v[48:51], v107, s[12:15], 0 offen sc1\n"
    "Lflc_5:\n"
    "s_waitcnt vmcnt(0)\n"
    "v_cmp_ne_u64 vcc, v[48:49], v[100:101]\n"
    "s_cbranch_vccnz Lfls_6\n"
    "Lfld_7:\n"
    "v_mov_b32_dpp v48, v50 quad_perm:[1,0,3,2] row_mask:0xf bank_mask:0xf\n"
    "v_cmp_ge_u32 vcc, v50, v48\n"
    "s_xor_b32 vcc_lo, vcc_lo, 0xaaaaaaaa\n"
    "s_bcnt1_i32_b32 s84, vcc_lo\n"
    "s_lshl_b32 s84, s84, 3\n"
    "s_cmp_gt_u32 s84, 128\n"
    "s_cselect_b32 s67, 0x2000, 0\n"
    "v_mov_b32 v224, s67\n"
    "ds_write_b32 v157, v224\n"
    "v_subrev_u32 v51, s11, v51\n"
    "s_nop 1\n"
    "v_max_i32_dpp v51, v51, v51 row_ror:1 row_mask:0xf bank_mask:0xf\n"
    "s_nop 1\n"
    "v_max_i32_dpp v51, v51, v51 row_ror:2 row_mask:0xf bank_mask:0xf\n"
    "s_nop 1\n"
    "v_max_i32_dpp v51, v51, v51 row_ror:4 row_mask:0xf bank_mask:0xf\n"
    "s_nop 1\n"
    "v_max_i32_dpp v51, v51, v51 row_ror:8 row_mask:0xf bank_mask:0xf\n"
    "v_mov_b32 v156, v51\n"
    "s_nop 1\n"
    "v_permlane16_swap_b32 v51, v156\n"
    "v_max_i32 v51, v51, v156\n"
    "v_mov_b32 v156, v51\n"
    "s_nop 1\n"
    "v_permlane32_swap_b32 v51, v156\n"
    "v_max_i32 v51, v51, v156\n"
    "s_nop 0\n"
    "v_readfirstlane_b32 s84, v51\n"
    "s_add_u32 s11, s11, s84\n"
    "v_mov_b32 v224, s11\n"
    "ds_write_b32 v157, v224 offset:4\n"
    "v_or_b32 v224, s56, v1\n"
    "v_cmp_eq_u32 vcc, 0, v224\n"
    "s_nop 1\n"
    "v_cndmask_b32 v224, v239, v98, vcc\n"
    "buffer_store_dwordx2 v[100:101], v224, s[12:15], 0 offen sc1\n"
    "v_subrev_u32 v222, 4096, v116\n"
    "s_mul_i32 s6, s65, 2112\n"
    "s_sub_u32 s5, 0, s6\n"
    "s_waitcnt lgkmcnt(0)\n"
    "Lwt_8:\n"
    "s_memrealtime s[78:79]\n"
    "s_waitcnt lgkmcnt(0)\n"
    "s_sub_u32 s78, s78, s11\n"
    "s_cmp_lt_u32 s78, 400\n"
    "s_cbranch_scc1 Lwt_8\n"
    "s_mov_b32 s87, 0xb0000\n"
    "s_add_u32 m0, s6, 135168\n"
    "s_nop 0\n"
    "buffer_load_dwordx4 v222, s[12:15], s87 offen offset:0 sc1 lds\n"
    "s_add_u32 m0, s6, 135200\n"
    "s_nop 0\n"
    "buffer_load_dwordx4 v222, s[12:15], s87 offen offset:1024 sc1 lds\n"
    "s_add_u32 m0, s5, 135232\n"
    "s_nop 0\n"
    "buffer_load_dwordx4 v222, s[12:15], s87 offen offset:2048 sc1 lds\n"
    "s_add_u32 m0, s5, 135264\n"
    "s_nop 0\n"
    "buffer_load_dwordx4 v222, s[12:15], s87 offen offset:3072 sc1 lds\n"
    "s_mov_b32 s87, 0xb1000\n"
    "s_add_u32 m0, s6, 139392\n"
    "s_nop 0\n"
    "buffer_load_dwordx4 v222, s[12:15], s87 offen offset:0 sc1 lds\n"
    "s_add_u32 m0, s6, 139424\n"
    "s_nop 0\n"
    "buffer_load_dwordx4 v222, s[12:15], s87 offen offset:1024 sc1 lds\n"
    "s_add_u32 m0, s5, 139456\n"
    "s_nop 0\n"
    "buffer_load_dwordx4 v222, s[12:15], s87 offen offset:2048 sc1 lds\n"
    "s_add_u32 m0, s5, 139488\n"
    "s_nop 0\n"
    "buffer_load_dwordx4 v222, s[12:15], s87 offen offset:3072 sc1 lds\n"
    "s_mov_b32 s87, 0xb4000\n"
    "s_add_u32 m0, s6, 143616\n"
    "s_nop 0\n"
    "buffer_load_dwordx4 v222, s[12:15], s87 offen offset:0 sc1 lds\n"
    "s_add_u32 m0, s6, 143648\n"
    "s_nop 0\n"
    "buffer_load_dwordx4 v222, s[12:15], s87 offen offset:1024 sc1 lds\n"
    "s_add_u32 m0, s5, 143680\n"
    "s_nop 0\n"
    "buffer_load_dwordx4 v222, s[12:15], s87 offen offset:2048 sc1 lds\n"
    "s_add_u32 m0, s5, 143712\n"
    "s_nop 0\n"
    "buffer_load_dwordx4 v222, s[12:15], s87 offen offset:3072 sc1 lds\n"
    "s_mov_b32 s87, 0xb5000\n"
    "s_add_u32 m0, s6, 147840\n"
    "s_nop 0\n"
    "buffer_load_dwordx4 v222, s[12:15], s87 offen offset:0 sc1 lds\n"
    "s_add_u32 m0, s6, 147872\n"
    "s_nop 0\n"
    "buffer_load_dwordx4 v222, s[12:15], s87 offen offset:1024 sc1 lds\n"
    "s_add_u32 m0, s5, 147904\n"
    "s_nop 0\n"
    "buffer_load_dwordx4 v222, s[12:15], s87 offen offset:2048 sc1 lds\n"
    "s_add_u32 m0, s5, 147936\n"
    "s_nop 0\n"
    "buffer_load_dwordx4 v222, s[12:15], s87 offen offset:3072 sc1 lds\n"
    "v_mov_b32 v226, 156672\n"
    "Lt0w0_12:\n"
    "ds_read_b64 v[224:225], v226 offset:24\n"
    "s_waitcnt lgkmcnt(0)\n"
    "v_or_b32 v224, v224, v225\n"
    "v_cmp_gt_i32 vcc, 0, v224\n"
    "s_cbranch_vccnz Lt0w0_12\n"
    "ds_read_b128 a[0:3], v89 offset:0\n"
    "ds_read_b128 a[4:7], v89 offset:64\n"
    "ds_read_b128 a[8:11], v89 offset:128\n"
    "ds_read_b128 a[12:15], v89 offset:192\n"
    "ds_read_b128 a[16:19], v89 offset:256\n"
    "ds_read_b128 a[20:23], v89 offset:320\n"
    "ds_read_b128 a[24:27], v89 offset:384\n"
    "ds_read_b128 a[28:31], v89 offset:448\n"
    "ds_read_b128 a[32:35], v89 offset:512\n"
    "ds_read_b128 a[36:39], v89 offset:576\n"
    "ds_read_b128 a[40:43], v89 offset:640\n"
    "ds_read_b128 a[44:47], v89 offset:704\n"
    "ds_read_b128 a[48:51], v89 offset:768\n"
    "ds_read_b128 a[52:55], v89 offset:832\n"
    "ds_read_b128 a[56:59], v89 offset:896\n"
    "ds_read_b128 a[60:63], v89 offset:960\n"
    "ds_read_b64_tr_b16 a[128:129], v90 offset:0\n"
    "ds_read_b64_tr_b16 a[132:133], v91 offset:0\n"
    "ds_read_b64_tr_b16 a[136:137], v92 offset:0\n"
    "ds_read_b64_tr_b16 a[140:141], v93 offset:0\n"
    "ds_read_b64_tr_b16 a[144:145], v90 offset:32\n"
    "ds_read_b64_tr_b16 a[148:149], v91 offset:32\n"
    "ds_read_b64_tr_b16 a[152:153], v92 offset:32\n"
    "ds_read_b64_tr_b16 a[156:157], v93 offset:32\n"
    "ds_read_b64_tr_b16 a[160:161], v90 offset:64\n"
    "ds_read_b64_tr_b16 a[164:165], v91 offset:64\n"
    "ds_read_b64_tr_b16 a[168:169], v92 offset:64\n"
    "ds_read_b64_tr_b16 a[172:173], v93 offset:64\n"
    "ds_read_b64_tr_b16 a[176:177], v90 offset:96\n"
    "ds_read_b64_tr_b16 a[180:181], v91 offset:96\n"
    "ds_read_b64_tr_b16 a[184:185], v92 offset:96\n"
    "ds_read_b64_tr_b16 a[188:189], v93 offset:96\n"
    "ds_read_b64_tr_b16 a[192:193], v90 offset:128\n"
    "ds_read_b64_tr_b16 a[196:197], v91 offset:128\n"
    "ds_read_b64_tr_b16 a[200:201], v92 offset:128\n"
    "ds_read_b64_tr_b16 a[204:205], v93 offset:128\n"
    "ds_read_b64_tr_b16 a[208:209], v90 offset:160\n"
    "ds_read_b64_tr_b16 a[212:213], v91 offset:160\n"
    "ds_read_b64_tr_b16 a[216:217], v92 offset:160\n"
    "ds_read_b64_tr_b16 a[220:221], v93 offset:160\n"
    "ds_read_b64_tr_b16 a[224:225], v90 offset:192\n"
    "ds_read_b64_tr_b16 a[228:229], v91 offset:192\n"
    "ds_read_b64_tr_b16 a[232:233], v92 offset:192\n"
    "ds_read_b64_tr_b16 a[236:237], v93 offset:192\n"
    "ds_read_b64_tr_b16 a[240:241], v90 offset:224\n"
    "ds_read_b64_tr_b16 a[244:245], v91 offset:224\n"
    "ds_read_b64_tr_b16 a[248:249], v92 offset:224\n"
    "ds_read_b64_tr_b16 a[252:253], v93 offset:224\n"
    "v_add_u32 v89, 67584, v89\n"
    "v_add_u32 v90, 67584, v90\n"
    "v_add_u32 v91, 67584, v91\n"
    "v_add_u32 v92, 67584, v92\n"
    "v_add_u32 v93, 67584, v93\n"
    "s_mov_b32 s87, 0x21000\n"
    "v_mul_u32_u24 v230, 1056, v2\n"
    "v_add_u32 v223, s87, v230\n"
    "v_lshl_add_u32 v223, v3, 7, v223\n"
    "s_waitcnt lgkmcnt(0)\n"
    "Lqpc_9:\n"
    "s_waitcnt vmcnt(0)\n"
    "ds_read2st64_b32 v[220:221], v223 offset0:0 offset1:2\n"
    "s_waitcnt lgkmcnt(0)\n"
    "v_max_u32 v220, v220, v221\n"
    "v_cmp_eq_u32 vcc, -1, v220\n"
    "s_cbranch_vccnz Lqps_10\n"
    "Lqpd_11:\n"
    "s_branch LbA\n"
    "Lld:\n"
    "s_lshl_b32 s5, s63, 12\n"
    "s_lshl_b32 s86, s62, 20\n"
    "s_add_u32 s5, s5, s86\n"
    "s_mul_i32 s84, s64, 1081344\n"
    "s_xor_b32 s5, s5, s84\n"
    "s_barrier\n"
    "s_mov_b32 s95, 0\n"
    "s_add_u32 m0, s6, 135168\n"
    "s_nop 0\n"
    "buffer_load_dwordx4 v155, s[16:19], s95 offen nt lds\n"
    "s_mov_b32 s95, 262144\n"
    "s_add_u32 m0, s6, 139392\n"
    "s_nop 0\n"
    "buffer_load_dwordx4 v155, s[16:19], s95 offen nt lds\n"
    "s_mov_b32 s95, 524288\n"
    "s_add_u32 m0, s6, 143616\n"
    "s_nop 0\n"
    "buffer_load_dwordx4 v155, s[16:19], s95 offen nt lds\n"
    "s_mov_b32 s95, 786432\n"
    "s_add_u32 m0, s6, 147840\n"
    "s_nop 0\n"
    "buffer_load_dwordx4 v155, s[16:19], s95 offen nt lds\n"
    "s_mov_b32 s95, 131072\n"
    "s_add_u32 m0, s6, 137280\n"
    "s_nop 0\n"
    "buffer_load_dwordx4 v155, s[16:19], s95 offen nt lds\n"
    "s_mov_b32 s95, 393216\n"
    "s_add_u32 m0, s6, 141504\n"
    "s_nop 0\n"
    "buffer_load_dwordx4 v155, s[16:19], s95 offen nt lds\n"
    "s_mov_b32 s95, 655360\n"
    "s_add_u32 m0, s6, 145728\n"
    "s_nop 0\n"
    "buffer_load_dwordx4 v155, s[16:19], s95 offen nt lds\n"
    "s_waitcnt lgkmcnt(0)\n"
    "s_mov_b32 s11, s2\n"
    "s_mul_i32 s98, s59, 4224\n"
    "s_mul_i32 s99, s59, 16896\n"
    "s_and_b32 s65, s63, 1\n"
    "v_xor_b32 v228, s59, v9\n"
    "v_xor_b32 v229, s59, v10\n"
    "s_mov_b32 s87, 0x21000\n"
    "v_mul_u32_u24 v230, 4224, v3\n"
    "v_add_u32 v99, s87, v230\n"
    "v_lshl_add_u32 v99, v9, 7, v99\n"
    "v_lshl_add_u32 v99, v10, 3, v99\n"
    "v_lshl_add_u32 v99, v228, 5, v99\n"
    "s_mov_b32 s87, 0x1ce00\n"
    "v_mul_u32_u24 v230, 1056, v9\n"
    "v_add_u32 v104, s87, v230\n"
    "v_lshl_add_u32 v104, v10, 8, v104\n"
    "v_lshl_add_u32 v104, v3, 4, v104\n"
    "v_lshl_add_u32 v104, v229, 6, v104\n"
    "s_lshl_b32 s84, s59, 10\n"
    "s_add_u32 s87, s84, 0x25200\n"
    "v_lshl_add_u32 v127, v1, 4, s87\n"
    "s_add_u32 s84, s84, s68\n"
    "v_lshl_add_u32 v122, v1, 4, s84\n"
    "v_lshl_add_u32 v228, v3, 6, 0\n"
    "v_lshl_add_u32 v228, v10, 4, v228\n"
    "v_lshl_add_u32 v229, v9, 8, 0\n"
    "v_lshl_add_u32 v229, v228, 0, v229\n"
    "s_bfe_u32 s85, s69, 0x30001\n"
    "s_lshl_b32 s4, s85, 14\n"
    "s_lshl_b32 s86, s64, 12\n"
    "s_add_u32 s4, s4, s86\n"
    "s_add_u32 s4, s4, s68\n"
    "s_lshl_b32 s84, s59, 10\n"
    "s_add_u32 s84, s84, s4\n"
    "s_add_u32 s87, s84, 0x40000\n"
    "v_lshl_add_u32 v113, v229, 0, s87\n"
    "v_lshrrev_b32 v226, 1, v3\n"
    "v_and_b32 v227, 1, v3\n"
    "s_bfe_u32 s85, s61, 0x10000\n"
    "s_lshl_b32 s84, s85, 11\n"
    "s_bfe_u32 s85, s59, 0x10001\n"
    "s_lshl_b32 s86, s85, 10\n"
    "s_add_u32 s84, s84, s86\n"
    "s_bfe_u32 s85, s61, 0x20001\n"
    "s_lshl_b32 s86, s85, 8\n"
    "s_add_u32 s84, s84, s86\n"
    "s_bfe_u32 s85, s59, 0x10000\n"
    "s_lshl_b32 s86, s85, 7\n"
    "s_add_u32 s84, s84, s86\n"
    "s_add_u32 s84, s84, s4\n"
    "s_add_u32 s87, s84, 0x40000\n"
    "v_lshl_add_u32 v112, v226, 6, s87\n"
    "v_lshl_add_u32 v112, v10, 4, v112\n"
    "v_lshl_add_u32 v112, v227, 3, v112\n"
    "s_lshl_b32 s84, s59, 10\n"
    "s_lshl_b32 s86, s61, 7\n"
    "s_add_u32 s84, s84, s86\n"
    "s_add_u32 s84, s84, s4\n"
    "v_lshl_add_u32 v123, v228, 0, s84\n"
    "v_cmp_eq_u32 vcc, 0, v9\n"
    "s_nop 1\n"
    "v_cndmask_b32 v112, v239, v112, vcc\n"
    "v_cndmask_b32 v123, v239, v123, vcc\n"
    "s_bfe_u32 s85, s57, 0x20000\n"
    "s_lshl_b32 s84, s85, 15\n"
    "s_bfe_u32 s85, s59, 0x10001\n"
    "s_lshl_b32 s86, s85, 14\n"
    "s_add_u32 s84, s84, s86\n"
    "s_bfe_u32 s85, s59, 0x10000\n"
    "s_lshl_b32 s86, s85, 12\n"
    "s_add_u32 s84, s84, s86\n"
    "s_add_u32 s84, s84, s68\n"
    "v_lshl_add_u32 v114, v229, 0, s84\n"
    "s_bfe_u32 s85, s59, 0x10000\n"
    "s_lshl_b32 s84, s85, 16\n"
    "s_lshl_b32 s86, s64, 15\n"
    "s_add_u32 s84, s84, s86\n"
    "s_bfe_u32 s85, s60, 0x10004\n"
    "s_lshl_b32 s86, s85, 14\n"
    "s_add_u32 s84, s84, s86\n"
    "s_bfe_u32 s85, s60, 0x30001\n"
    "s_lshl_b32 s86, s85, 10\n"
    "s_add_u32 s84, s84, s86\n"
    "s_lshl_b32 s86, s61, 7\n"
    "s_add_u32 s84, s84, s86\n"
    "s_bfe_u32 s85, s59, 0x10001\n"
    "s_lshl_b32 s86, s85, 13\n"
    "s_add_u32 s84, s84, s86\n"
    "s_add_u32 s87, s84, 0x60000\n"
    "v_lshl_add_u32 v115, v1, 1, s87\n"
    "v_lshl_or_b32 v115, v1, 31, v115\n"
    "s_bfe_u32 s4, s60, 0x20003\n"
    "s_xor_b32 s4, s4, s59\n"
    "s_lshl_b32 s84, s69, 18\n"
    "s_lshl_b32 s86, s61, 15\n"
    "s_add_u32 s84, s84, s86\n"
    "s_bfe_u32 s85, s4, 0x10001\n"
    "s_lshl_b32 s86, s85, 14\n"
    "s_add_u32 s84, s84, s86\n"
    "s_bfe_u32 s85, s4, 0x10000\n"
    "s_lshl_b32 s86, s85, 12\n"
    "s_add_u32 s84, s84, s86\n"
    "s_bfe_u32 s85, s60, 0x10001\n"
    "s_lshl_b32 s4, s85, 5\n"
    "s_bfe_u32 s85, s69, 0x10003\n"
    "s_lshl_b32 s85, s85, 13\n"
    "s_xor_b32 s85, s85, s68\n"
    "s_add_u32 s84, s84, s85\n"
    "v_lshlrev_b32 v224, 1, v3\n"
    "v_xor_b32 v224, v224, v1\n"
    "v_xor_b32 v224, s4, v224\n"
    "s_add_u32 s87, s84, 0x400000\n"
    "v_lshl_add_u32 v118, v224, 4, s87\n"
    "s_lshl_b32 s84, s59, 6\n"
    "v_and_b32 v225, 1, v3\n"
    "v_lshrrev_b32 v226, 1, v9\n"
    "v_and_b32 v227, 7, v2\n"
    "s_add_u32 s87, s84, 0x26300\n"
    "v_lshl_add_u32 v135, v9, 4, s87\n"
    "v_lshl_add_u32 v135, v225, 3, v135\n"
    "v_lshl_add_u32 v135, v10, 1, v135\n"
    "s_add_u32 s87, s84, 0x26300\n"
    "v_lshl_add_u32 v136, v3, 4, s87\n"
    "s_mov_b32 s87, 0x21000\n"
    "v_mul_u32_u24 v230, 1056, v2\n"
    "v_add_u32 v124, s87, v230\n"
    "v_lshl_add_u32 v124, v3, 4, v124\n"
    "s_lshl_b32 s84, s59, 2\n"
    "s_add_u32 s87, s84, 0x26200\n"
    "v_lshl_add_u32 v125, v2, 4, s87\n"
    "s_mov_b32 s87, 0x26200\n"
    "v_lshl_add_u32 v126, v2, 4, s87\n"
    "s_add_u32 s84, s59, 1\n"
    "s_and_b32 s84, s84, 3\n"
    "s_lshl_b32 s84, s84, 10\n"
    "s_add_u32 s87, s84, 0x25200\n"
    "v_lshl_add_u32 v128, v1, 4, s87\n"
    "s_add_u32 s84, s59, 2\n"
    "s_and_b32 s84, s84, 3\n"
    "s_lshl_b32 s84, s84, 10\n"
    "s_add_u32 s87, s84, 0x25200\n"
    "v_lshl_add_u32 v129, v1, 4, s87\n"
    "s_add_u32 s84, s59, 3\n"
    "s_and_b32 s84, s84, 3\n"
    "s_lshl_b32 s84, s84, 10\n"
    "s_add_u32 s87, s84, 0x25200\n"
    "v_lshl_add_u32 v130, v1, 4, s87\n"
    "s_lshl_b32 s84, s59, 13\n"
    "s_add_u32 s87, s84, 0x10800\n"
    "s_mov_b32 s95, 917504\n"
    "s_add_u32 m0, s6, 149952\n"
    "s_waitcnt vmcnt(6)\n"
    "buffer_load_dwordx4 v155, s[16:19], s95 offen nt lds\n"
    "s_mov_b32 s95, 0\n"
    "s_add_u32 m0, s6, 118272\n"
    "s_waitcnt vmcnt(6)\n"
    "buffer_load_dwordx4 v156, s[24:27], s95 offen nt lds\n"
    "s_mov_b32 s95, 65536\n"
    "s_add_u32 m0, s6, 120384\n"
    "s_waitcnt vmcnt(6)\n"
    "buffer_load_dwordx4 v156, s[24:27], s95 offen nt lds\n"
    "s_mov_b32 s95, 131072\n"
    "s_add_u32 m0, s6, 122496\n"
    "s_waitcnt vmcnt(6)\n"
    "s_barrier\n"
    "buffer_load_dwordx4 v156, s[24:27], s95 offen nt lds\n"
    "s_mov_b32 s95, 196608\n"
    "s_add_u32 m0, s6, 124608\n"
    "s_waitcnt vmcnt(6)\n"
    "buffer_load_dwordx4 v156, s[24:27], s95 offen nt lds\n"
    "s_mov_b32 s95, 262144\n"
    "s_add_u32 m0, s6, 126720\n"
    "s_waitcnt vmcnt(6)\n"
    "buffer_load_dwordx4 v156, s[24:27], s95 offen nt lds\n"
    "s_mov_b32 s95, 327680\n"
    "s_add_u32 m0, s6, 128832\n"
    "s_waitcnt vmcnt(6)\n"
    "buffer_load_dwordx4 v156, s[24:27], s95 offen nt lds\n"
    "s_mov_b32 s95, 393216\n"
    "s_add_u32 m0, s6, 130944\n"
    "s_waitcnt vmcnt(6)\n"
    "s_barrier\n"
    "buffer_load_dwordx4 v156, s[24:27], s95 offen nt lds\n"
    "s_mov_b32 s95, 458752\n"
    "s_add_u32 m0, s6, 133056\n"
    "s_waitcnt vmcnt(6)\n"
    "buffer_load_dwordx4 v156, s[24:27], s95 offen nt lds\n"
    "s_xor_b32 s95, s5, 0\n"
    "s_add_u32 m0, s6, 0\n"
    "s_waitcnt vmcnt(6)\n"
    "buffer_load_dwordx4 v83, s[28:31], s95 offen offset:0 lds\n"
    "s_add_u32 m0, s6, 64\n"
    "s_waitcnt vmcnt(6)\n"
    "buffer_load_dwordx4 v83, s[28:31], s95 offen offset:2048 lds\n"
    "s_xor_b32 s95, s5, 135168\n"
    "s_add_u32 m0, s6, 4224\n"
    "s_waitcnt vmcnt(6)\n"
    "buffer_load_dwordx4 v83, s[28:31], s95 offen offset:0 lds\n"
    "s_add_u32 m0, s6, 4288\n"
    "s_waitcnt vmcnt(6)\n"
    "buffer_load_dwordx4 v83, s[28:31], s95 offen offset:2048 lds\n"
    "s_xor_b32 s95, s5, 270336\n"
    "s_add_u32 m0, s6, 8448\n"
    "s_waitcnt vmcnt(6)\n"
    "buffer_load_dwordx4 v83, s[28:31], s95 offen offset:0 lds\n"
    "s_add_u32 m0, s6, 8512\n"
    "s_waitcnt vmcnt(6)\n"
    "buffer_load_dwordx4 v83, s[28:31], s95 offen offset:2048 lds\n"
    "s_xor_b32 s95, s5, 405504\n"
    "s_add_u32 m0, s6, 12672\n"
    "s_waitcnt vmcnt(6)\n"
    "s_barrier\n"
    "buffer_load_dwordx4 v83, s[28:31], s95 offen offset:0 lds\n"
    "ds_read_b64_tr_b16 v[16:17], v99 offset:0\n"
    "ds_read_b64_tr_b16 v[18:19], v99 offset:512\n"
    "ds_read_b64_tr_b16 v[20:21], v99 offset:1056\n"
    "s_add_u32 m0, s6, 12736\n"
    "s_waitcnt vmcnt(7)\n"
    "buffer_load_dwordx4 v83, s[28:31], s95 offen offset:2048 lds\n"
    "ds_read_b64_tr_b16 v[22:23], v99 offset:1568\n"
    "ds_read_b64_tr_b16 v[24:25], v99 offset:2112\n"
    "ds_read_b64_tr_b16 v[26:27], v99 offset:2624\n"
    "s_xor_b32 s95, s5, 540672\n"
    "s_add_u32 m0, s6, 16896\n"
    "s_waitcnt vmcnt(8)\n"
    "buffer_load_dwordx4 v83, s[28:31], s95 offen offset:0 lds\n"
    "ds_read_b64_tr_b16 v[28:29], v99 offset:3168\n"
    "ds_read_b64_tr_b16 v[30:31], v99 offset:3680\n"
    "ds_read_b128 v[32:35], v104 offset:0\n"
    "s_add_u32 m0, s6, 16960\n"
    "s_waitcnt vmcnt(9)\n"
    "buffer_load_dwordx4 v83, s[28:31], s95 offen offset:2048 lds\n"
    "ds_read_b128 v[36:39], v104 offset:4224\n"
    "ds_read_b128 v[40:43], v104 offset:8448\n"
    "ds_read_b128 v[44:47], v104 offset:12672\n"
    "s_xor_b32 s95, s5, 675840\n"
    "s_add_u32 m0, s6, 21120\n"
    "s_waitcnt vmcnt(10)\n"
    "buffer_load_dwordx4 v83, s[28:31], s95 offen offset:0 lds\n"
    "s_waitcnt lgkmcnt(0)\n"
    "s_add_u32 m0, s6, 21184\n"
    "s_waitcnt vmcnt(11)\n"
    "s_barrier\n"
    "buffer_load_dwordx4 v83, s[28:31], s95 offen offset:2048 lds\n"
    "v_lshl_add_u32 v131, v3, 9, s87\n"
    "v_lshl_add_u32 v131, v9, 7, v131\n"
    "v_lshl_add_u32 v131, v225, 6, v131\n"
    "v_lshl_add_u32 v131, v226, 5, v131\n"
    "v_lshl_add_u32 v131, v10, 3, v131\n"
    "v_xor_b32 v132, 32, v131\n"
    "s_xor_b32 s95, s5, 811008\n"
    "s_add_u32 m0, s6, 25344\n"
    "s_waitcnt vmcnt(12)\n"
    "buffer_load_dwordx4 v83, s[28:31], s95 offen offset:0 lds\n"
    "v_xor_b32 v133, 64, v131\n"
    "v_xor_b32 v134, 96, v131\n"
    "s_lshl_b32 s84, s59, 4\n"
    "s_add_u32 s87, s84, 0x26a00\n"
    "v_lshl_add_u32 v137, v3, 6, s87\n"
    "s_add_u32 s87, s84, 0x26440\n"
    "s_add_u32 m0, s6, 25408\n"
    "s_waitcnt vmcnt(13)\n"
    "buffer_load_dwordx4 v83, s[28:31], s95 offen offset:2048 lds\n"
    "v_mov_b32 v143, s87\n"
    "s_mov_b32 s87, 0x26a00\n"
    "v_lshl_add_u32 v138, v3, 6, s87\n"
    "v_lshl_add_u32 v138, v10, 4, v138\n"
    "s_lshl_b32 s84, s59, 8\n"
    "s_add_u32 s87, s84, 0x25200\n"
    "s_xor_b32 s95, s5, 946176\n"
    "s_add_u32 m0, s6, 29568\n"
    "s_waitcnt vmcnt(13)\n"
    "buffer_load_dwordx4 v83, s[28:31], s95 offen offset:0 lds\n"
    "v_lshl_add_u32 v141, v1, 2, s87\n"
    "s_lshl_b32 s84, s59, 10\n"
    "s_add_u32 s87, s84, 0x25200\n"
    "v_lshl_add_u32 v139, v1, 2, s87\n"
    "s_mov_b32 s87, 0x25200\n"
    "v_lshl_add_u32 v140, v1, 2, s87\n"
    "s_add_u32 m0, s6, 29632\n"
    "s_waitcnt vmcnt(13)\n"
    "buffer_load_dwordx4 v83, s[28:31], s95 offen offset:2048 lds\n"
    "s_lshl_b32 s84, s59, 2\n"
    "v_lshrrev_b32 v224, 1, v3\n"
    "s_add_u32 s87, s84, 0x26440\n"
    "v_lshl_add_u32 v142, v224, 6, s87\n"
    "v_lshl_add_u32 v142, v10, 4, v142\n"
    "v_and_b32 v224, 1, v1\n"
    "s_xor_b32 s95, s5, 1081344\n"
    "s_add_u32 m0, s6, 33792\n"
    "s_waitcnt vmcnt(13)\n"
    "buffer_load_dwordx4 v83, s[28:31], s95 offen offset:0 lds\n"
    "v_lshl_add_u32 v121, v226, 6, 0\n"
    "v_lshl_add_u32 v121, v3, 4, v121\n"
    "v_lshl_add_u32 v121, v227, 1, v121\n"
    "v_mul_u32_u24 v230, 8190, v224\n"
    "v_add_u32 v121, v121, v230\n"
    "s_bfe_u32 s85, s58, 0x30002\n"
    "s_add_u32 m0, s6, 33856\n"
    "s_waitcnt vmcnt(13)\n"
    "buffer_load_dwordx4 v83, s[28:31], s95 offen offset:2048 lds\n"
    "s_lshl_b32 s97, s85, 1\n"
    "s_add_u32 s97, s97, s64\n"
    "s_bfe_u32 s85, s62, 0x10000\n"
    "s_lshl_b32 s84, s85, 16\n"
    "s_lshl_b32 s86, s64, 15\n"
    "s_add_u32 s84, s84, s86\n"
    "s_xor_b32 s95, s5, 1216512\n"
    "s_add_u32 m0, s6, 38016\n"
    "s_waitcnt vmcnt(13)\n"
    "buffer_load_dwordx4 v83, s[28:31], s95 offen offset:0 lds\n"
    "s_bfe_u32 s85, s59, 0x10001\n"
    "s_lshl_b32 s86, s85, 14\n"
    "s_add_u32 s84, s84, s86\n"
    "s_bfe_u32 s85, s59, 0x10000\n"
    "s_lshl_b32 s86, s85, 12\n"
    "s_add_u32 s84, s84, s86\n"
    "s_add_u32 m0, s6, 38080\n"
    "s_waitcnt vmcnt(13)\n"
    "buffer_load_dwordx4 v83, s[28:31], s95 offen offset:2048 lds\n"
    "s_add_u32 s84, s84, s68\n"
    "s_add_u32 s87, s84, 0x60000\n"
    "v_lshl_add_u32 v116, v1, 4, s87\n"
    "s_lshl_b32 s84, s64, 18\n"
    "s_lshl_b32 s86, s59, 16\n"
    "s_add_u32 s84, s84, s86\n"
    "s_xor_b32 s95, s5, 1351680\n"
    "s_add_u32 m0, s6, 42240\n"
    "s_waitcnt vmcnt(13)\n"
    "buffer_load_dwordx4 v83, s[28:31], s95 offen offset:0 lds\n"
    "v_lshrrev_b32 v224, 1, v3\n"
    "v_xor_b32 v224, v224, v225\n"
    "v_xor_b32 v228, s62, v3\n"
    "v_and_b32 v229, 1, v228\n"
    "v_lshrrev_b32 v228, 1, v228\n"
    "s_add_u32 s87, s84, 0x400000\n"
    "s_add_u32 m0, s6, 42304\n"
    "s_waitcnt vmcnt(13)\n"
    "buffer_load_dwordx4 v83, s[28:31], s95 offen offset:2048 lds\n"
    "v_lshl_add_u32 v117, v225, 21, s87\n"
    "v_lshl_add_u32 v117, v224, 13, v117\n"
    "v_lshl_add_u32 v117, v2, 3, v117\n"
    "v_lshl_add_u32 v117, v229, 12, v117\n"
    "v_lshl_add_u32 v117, v228, 14, v117\n"
    "s_lshl_b32 s4, s59, 3\n"
    "s_xor_b32 s95, s5, 1486848\n"
    "s_add_u32 m0, s6, 46464\n"
    "s_waitcnt vmcnt(13)\n"
    "buffer_load_dwordx4 v83, s[28:31], s95 offen offset:0 lds\n"
    "s_bfe_u32 s85, s63, 0x40001\n"
    "s_xor_b32 s4, s4, s85\n"
    "s_lshl_b32 s84, s65, 20\n"
    "s_lshl_b32 s86, s4, 7\n"
    "s_add_u32 s84, s84, s86\n"
    "s_xor_b32 s70, s84, 0x0\n"
    "s_add_u32 m0, s6, 46528\n"
    "s_waitcnt vmcnt(13)\n"
    "buffer_load_dwordx4 v83, s[28:31], s95 offen offset:2048 lds\n"
    "s_xor_b32 s71, s84, 0x80200\n"
    "s_xor_b32 s72, s84, 0x100800\n"
    "s_xor_b32 s73, s84, 0x180a00\n"
    "s_xor_b32 s74, s84, 0x8000\n"
    "s_xor_b32 s75, s84, 0x88200\n"
    "s_xor_b32 s76, s84, 0x108800\n"
    "s_xor_b32 s95, s5, 1622016\n"
    "s_add_u32 m0, s6, 50688\n"
    "s_waitcnt vmcnt(13)\n"
    "buffer_load_dwordx4 v83, s[28:31], s95 offen offset:0 lds\n"
    "s_xor_b32 s77, s84, 0x188a00\n"
    "s_lshl_b32 s84, s62, 14\n"
    "s_lshl_b32 s86, s64, 12\n"
    "s_add_u32 s84, s84, s86\n"
    "s_lshl_b32 s86, s63, 7\n"
    "s_add_u32 s84, s84, s86\n"
    "s_add_u32 m0, s6, 50752\n"
    "s_waitcnt vmcnt(13)\n"
    "buffer_load_dwordx4 v83, s[28:31], s95 offen offset:2048 lds\n"
    "s_cmp_eq_u32 s59, 0\n"
    "s_cselect_b32 s84, s84, 0x80000000\n"
    "s_lshl_b32 s85, s65, 1\n"
    "v_xor_b32 v224, s85, v2\n"
    "s_add_u32 s87, s84, 0x80000\n"
    "v_lshl_add_u32 v119, v224, 3, s87\n"
    "s_xor_b32 s95, s5, 1757184\n"
    "s_add_u32 m0, s6, 54912\n"
    "s_waitcnt vmcnt(13)\n"
    "buffer_load_dwordx4 v83, s[28:31], s95 offen offset:0 lds\n"
    "v_lshl_add_u32 v119, v3, 13, v119\n"
    "v_cmp_gt_u32 vcc, 2, v3\n"
    "s_nop 1\n"
    "v_cndmask_b32 v119, v239, v119, vcc\n"
    "s_bfe_u32 s4, s60, 0x10002\n"
    "s_lshl_b32 s84, s59, 14\n"
    "s_add_u32 m0, s6, 54976\n"
    "s_waitcnt vmcnt(13)\n"
    "buffer_load_dwordx4 v83, s[28:31], s95 offen offset:2048 lds\n"
    "s_add_u32 s84, s84, s68\n"
    "s_lshl_b32 s86, s64, 12\n"
    "s_add_u32 s84, s84, s86\n"
    "s_bfe_u32 s85, s60, 0x40001\n"
    "s_lshl_b32 s86, s85, 3\n"
    "s_add_u32 s84, s84, s86\n"
    "s_xor_b32 s95, s5, 1892352\n"
    "s_add_u32 m0, s6, 59136\n"
    "s_waitcnt vmcnt(13)\n"
    "buffer_load_dwordx4 v83, s[28:31], s95 offen offset:0 lds\n"
    "v_xor_b32 v224, s4, v225\n"
    "s_add_u32 s87, s84, 0x80000\n"
    "v_lshl_add_u32 v120, v2, 8, s87\n"
    "v_lshl_add_u32 v120, v224, 7, v120\n"
    "s_mov_b32 s83, 0xb0000\n"
    "s_mul_i32 s84, s56, 1536\n"
    "s_add_u32 m0, s6, 59200\n"
    "s_waitcnt vmcnt(13)\n"
    "buffer_load_dwordx4 v83, s[28:31], s95 offen offset:2048 lds\n"
    "s_add_u32 s87, s84, 0x90000\n"
    "v_lshl_add_u32 v80, v0, 4, s87\n"
    "v_cmp_gt_u32 vcc, 96, v0\n"
    "s_nop 1\n"
    "v_cndmask_b32 v80, v239, v80, vcc\n"
    "v_lshrrev_b32 v228, 1, v1\n"
    "s_xor_b32 s95, s5, 2027520\n"
    "s_add_u32 m0, s6, 63360\n"
    "s_waitcnt vmcnt(13)\n"
    "buffer_load_dwordx4 v83, s[28:31], s95 offen offset:0 lds\n"
    "v_and_b32 v229, 1, v1\n"
    "s_lshl_b32 s84, s58, 7\n"
    "s_lshl_b32 s86, s57, 2\n"
    "s_add_u32 s84, s84, s86\n"
    "s_add_u32 s87, s84, 0x140000\n"
    "v_lshl_add_u32 v88, v0, 13, s87\n"
    "s_add_u32 m0, s6, 63424\n"
    "s_waitcnt vmcnt(13)\n"
    "buffer_load_dwordx4 v83, s[28:31], s95 offen offset:2048 lds\n"
    "v_cmp_gt_u32 vcc, 2, v0\n"
    "s_nop 1\n"
    "v_cndmask_b32 v88, v239, v88, vcc\n"
    "s_add_u32 s87, s68, 0x140000\n"
    "v_lshl_add_u32 v236, v228, 7, s87\n"
    "v_lshl_add_u32 v236, v229, 4, v236\n"
    "s_xor_b32 s95, s5, 2162688\n"
    "s_add_u32 m0, s6, 67584\n"
    "s_waitcnt vmcnt(13)\n"
    "buffer_load_dwordx4 v83, s[28:31], s95 offen offset:0 lds\n"
    "v_mul_u32_u24 v230, 1056, v2\n"
    "v_add_u32 v89, s99, v230\n"
    "v_lshl_add_u32 v89, v3, 4, v89\n"
    "v_mul_u32_u24 v230, 4224, v3\n"
    "v_add_u32 v229, 0, v230\n"
    "v_mul_u32_u24 v230, 1056, v9\n"
    "s_add_u32 m0, s6, 67648\n"
    "s_waitcnt vmcnt(13)\n"
    "buffer_load_dwordx4 v83, s[28:31], s95 offen offset:2048 lds\n"
    "v_add_u32 v229, v229, v230\n"
    "v_lshl_add_u32 v229, v10, 3, v229\n"
    "s_add_u32 s84, s59, 0\n"
    "s_and_b32 s84, s84, 3\n"
    "s_mul_i32 s84, s84, 16896\n"
    "s_lshl_b32 s86, s59, 8\n"
    "s_xor_b32 s95, s5, 2297856\n"
    "s_add_u32 m0, s6, 71808\n"
    "s_waitcnt vmcnt(13)\n"
    "buffer_load_dwordx4 v83, s[28:31], s95 offen offset:0 lds\n"
    "s_add_u32 s84, s84, s86\n"
    "v_add_u32 v90, s84, v229\n"
    "s_add_u32 s84, s59, 1\n"
    "s_and_b32 s84, s84, 3\n"
    "s_mul_i32 s84, s84, 16896\n"
    "s_lshl_b32 s86, s59, 8\n"
    "s_add_u32 m0, s6, 71872\n"
    "s_waitcnt vmcnt(13)\n"
    "buffer_load_dwordx4 v83, s[28:31], s95 offen offset:2048 lds\n"
    "s_add_u32 s84, s84, s86\n"
    "v_add_u32 v91, s84, v229\n"
    "s_add_u32 s84, s59, 2\n"
    "s_and_b32 s84, s84, 3\n"
    "s_mul_i32 s84, s84, 16896\n"
    "s_lshl_b32 s86, s59, 8\n"
    "s_xor_b32 s95, s5, 2433024\n"
    "s_add_u32 m0, s6, 76032\n"
    "s_waitcnt vmcnt(13)\n"
    "buffer_load_dwordx4 v83, s[28:31], s95 offen offset:0 lds\n"
    "s_add_u32 s84, s84, s86\n"
    "v_add_u32 v92, s84, v229\n"
    "s_add_u32 s84, s59, 3\n"
    "s_and_b32 s84, s84, 3\n"
    "s_mul_i32 s84, s84, 16896\n"
    "s_lshl_b32 s86, s59, 8\n"
    "s_add_u32 m0, s6, 76096\n"
    "s_waitcnt vmcnt(13)\n"
    "buffer_load_dwordx4 v83, s[28:31], s95 offen offset:2048 lds\n"
    "s_add_u32 s84, s84, s86\n"
    "v_add_u32 v93, s84, v229\n"
    "v_lshrrev_b32 v228, 1, v0\n"
    "s_lshl_b32 s84, s57, 19\n"
    "s_lshl_b32 s86, s97, 15\n"
    "s_add_u32 s84, s84, s86\n"
    "s_xor_b32 s95, s5, 2568192\n"
    "s_add_u32 m0, s6, 80256\n"
    "s_waitcnt vmcnt(13)\n"
    "buffer_load_dwordx4 v83, s[28:31], s95 offen offset:0 lds\n"
    "v_lshl_add_u32 v244, v228, 7, s84\n"
    "v_add_u32 v245, 0x4000, v244\n"
    "s_mul_i32 s84, s57, 7\n"
    "s_lshr_b32 s84, s84, 1\n"
    "s_lshl_b32 s84, s84, 7\n"
    "s_mul_i32 s86, s97, 114688\n"
    "s_add_u32 m0, s6, 80320\n"
    "s_waitcnt vmcnt(13)\n"
    "buffer_load_dwordx4 v83, s[28:31], s95 offen offset:2048 lds\n"
    "s_add_u32 s84, s84, s86\n"
    "v_lshrrev_b32 v229, 3, v0\n"
    "v_and_b32 v227, 3, v228\n"
    "v_mul_u32_u24 v230, 3584, v229\n"
    "v_add_u32 v246, s84, v230\n"
    "v_lshl_add_u32 v246, v227, 7, v246\n"
    "s_xor_b32 s95, s5, 2703360\n"
    "s_add_u32 m0, s6, 84480\n"
    "s_waitcnt vmcnt(13)\n"
    "buffer_load_dwordx4 v83, s[28:31], s95 offen offset:0 lds\n"
    "v_min_u32 v228, 111, v228\n"
    "s_lshl_b32 s84, s57, 7\n"
    "s_mul_i32 s86, s97, 114688\n"
    "s_add_u32 s84, s84, s86\n"
    "v_lshl_add_u32 v247, v228, 10, s84\n"
    "s_lshl_b32 s84, s57, 19\n"
    "s_add_u32 m0, s6, 84544\n"
    "s_waitcnt vmcnt(13)\n"
    "buffer_load_dwordx4 v83, s[28:31], s95 offen offset:2048 lds\n"
    "s_lshl_b32 s86, s97, 15\n"
    "s_add_u32 s84, s84, s86\n"
    "s_lshl_b32 s86, s59, 12\n"
    "s_add_u32 s84, s84, s86\n"
    "v_lshl_add_u32 v228, v1, 4, s84\n"
    "v_mov_b32 v229, s61\n"
    "s_xor_b32 s95, s5, 2838528\n"
    "s_add_u32 m0, s6, 88704\n"
    "s_waitcnt vmcnt(13)\n"
    "buffer_load_dwordx4 v83, s[28:31], s95 offen offset:0 lds\n"
    "v_and_b32 v229, 1, v229\n"
    "v_cmp_eq_u32 vcc, 0, v229\n"
    "s_nop 1\n"
    "v_cndmask_b32 v82, v228, v82, vcc\n"
    "s_add_u32 m0, s6, 88768\n"
    "s_waitcnt vmcnt(13)\n"
    "buffer_load_dwordx4 v83, s[28:31], s95 offen offset:2048 lds\n"
    "s_xor_b32 s95, s5, 2973696\n"
    "s_add_u32 m0, s6, 92928\n"
    "s_waitcnt vmcnt(13)\n"
    "buffer_load_dwordx4 v83, s[28:31], s95 offen offset:0 lds\n"
    "s_add_u32 m0, s6, 92992\n"
    "s_waitcnt vmcnt(13)\n"
    "buffer_load_dwordx4 v83, s[28:31], s95 offen offset:2048 lds\n"
    "s_lshl_b32 s84, s59, 2\n"
    "s_add_u32 s87, s84, 0x26410\n"
    "v_mov_b32 v224, s87\n"
    "ds_write_b32 v224, v1\n"
    "s_waitcnt lgkmcnt(0)\n"
    "s_xor_b32 s95, s5, 3108864\n"
    "s_add_u32 m0, s6, 97152\n"
    "s_waitcnt vmcnt(13)\n"
    "buffer_load_dwordx4 v83, s[28:31], s95 offen offset:0 lds\n"
    "s_add_u32 m0, s6, 97216\n"
    "s_waitcnt vmcnt(13)\n"
    "buffer_load_dwordx4 v83, s[28:31], s95 offen offset:2048 lds\n"
    "v_mov_b32 v226, 156672\n"
    "Lt0w1_1:\n"
    "ds_read_b64 v[224:225], v226 offset:24\n"
    "s_waitcnt lgkmcnt(0)\n"
    "v_or_b32 v224, v224, v225\n"
    "v_cmp_gt_i32 vcc, 0, v224\n"
    "s_cbranch_vccnz Lt0w1_1\n"
    "s_xor_b32 s95, s5, 3244032\n"
    "s_add_u32 m0, s6, 101376\n"
    "s_waitcnt vmcnt(13)\n"
    "buffer_load_dwordx4 v83, s[28:31], s95 offen offset:0 lds\n"
    "ds_read_b128 a[0:3], v89 offset:0\n"
    "ds_read_b128 a[4:7], v89 offset:64\n"
    "ds_read_b128 a[8:11], v89 offset:128\n"
    "ds_read_b128 a[12:15], v89 offset:192\n"
    "s_add_u32 m0, s6, 101440\n"
    "s_waitcnt vmcnt(13)\n"
    "buffer_load_dwordx4 v83, s[28:31], s95 offen offset:2048 lds\n"
    "ds_read_b128 a[16:19], v89 offset:256\n"
    "ds_read_b128 a[20:23], v89 offset:320\n"
    "ds_read_b128 a[24:27], v89 offset:384\n"
    "ds_read_b128 a[28:31], v89 offset:448\n"
    "s_xor_b32 s95, s5, 3379200\n"
    "s_add_u32 m0, s6, 105600\n"
    "s_waitcnt vmcnt(13)\n"
    "buffer_load_dwordx4 v83, s[28:31], s95 offen offset:0 lds\n"
    "ds_read_b128 a[32:35], v89 offset:512\n"
    "ds_read_b128 a[36:39], v89 offset:576\n"
    "ds_read_b128 a[40:43], v89 offset:640\n"
    "ds_read_b128 a[44:47], v89 offset:704\n"
    "s_add_u32 m0, s6, 105664\n"
    "s_waitcnt vmcnt(13)\n"
    "buffer_load_dwordx4 v83, s[28:31], s95 offen offset:2048 lds\n"
    "ds_read_b128 a[48:51], v89 offset:768\n"
    "ds_read_b128 a[52:55], v89 offset:832\n"
    "ds_read_b128 a[56:59], v89 offset:896\n"
    "ds_read_b128 a[60:63], v89 offset:960\n"
    "s_xor_b32 s95, s5, 3514368\n"
    "s_add_u32 m0, s6, 109824\n"
    "s_waitcnt vmcnt(13)\n"
    "buffer_load_dwordx4 v83, s[28:31], s95 offen offset:0 lds\n"
    "ds_read_b64_tr_b16 a[128:129], v90 offset:0\n"
    "ds_read_b64_tr_b16 a[132:133], v91 offset:0\n"
    "ds_read_b64_tr_b16 a[136:137], v92 offset:0\n"
    "ds_read_b64_tr_b16 a[140:141], v93 offset:0\n"
    "s_add_u32 m0, s6, 109888\n"
    "s_waitcnt vmcnt(13)\n"
    "buffer_load_dwordx4 v83, s[28:31], s95 offen offset:2048 lds\n"
    "ds_read_b64_tr_b16 a[144:145], v90 offset:32\n"
    "ds_read_b64_tr_b16 a[148:149], v91 offset:32\n"
    "ds_read_b64_tr_b16 a[152:153], v92 offset:32\n"
    "ds_read_b64_tr_b16 a[156:157], v93 offset:32\n"
    "s_xor_b32 s95, s5, 3649536\n"
    "s_add_u32 m0, s6, 114048\n"
    "s_waitcnt vmcnt(13)\n"
    "buffer_load_dwordx4 v83, s[28:31], s95 offen offset:0 lds\n"
    "ds_read_b64_tr_b16 a[160:161], v90 offset:64\n"
    "ds_read_b64_tr_b16 a[164:165], v91 offset:64\n"
    "ds_read_b64_tr_b16 a[168:169], v92 offset:64\n"
    "ds_read_b64_tr_b16 a[172:173], v93 offset:64\n"
    "s_add_u32 m0, s6, 114112\n"
    "s_waitcnt vmcnt(13)\n"
    "buffer_load_dwordx4 v83, s[28:31], s95 offen offset:2048 lds\n"
    "ds_read_b64_tr_b16 a[176:177], v90 offset:96\n"
    "ds_read_b64_tr_b16 a[180:181], v91 offset:96\n"
    "ds_read_b64_tr_b16 a[184:185], v92 offset:96\n"
    "ds_read_b64_tr_b16 a[188:189], v93 offset:96\n"
    "s_xor_b32 s95, s5, 3784704\n"
    "s_add_u32 m0, s6, 118272\n"
    "s_waitcnt vmcnt(13)\n"
    "buffer_load_dwordx4 v83, s[28:31], s95 offen offset:0 lds\n"
    "ds_read_b64_tr_b16 a[192:193], v90 offset:128\n"
    "ds_read_b64_tr_b16 a[196:197], v91 offset:128\n"
    "ds_read_b64_tr_b16 a[200:201], v92 offset:128\n"
    "ds_read_b64_tr_b16 a[204:205], v93 offset:128\n"
    "s_add_u32 m0, s6, 118336\n"
    "s_waitcnt vmcnt(13)\n"
    "buffer_load_dwordx4 v83, s[28:31], s95 offen offset:2048 lds\n"
    "ds_read_b64_tr_b16 a[208:209], v90 offset:160\n"
    "ds_read_b64_tr_b16 a[212:213], v91 offset:160\n"
    "ds_read_b64_tr_b16 a[216:217], v92 offset:160\n"
    "ds_read_b64_tr_b16 a[220:221], v93 offset:160\n"
    "s_xor_b32 s95, s5, 3919872\n"
    "s_add_u32 m0, s6, 122496\n"
    "s_waitcnt vmcnt(13)\n"
    "buffer_load_dwordx4 v83, s[28:31], s95 offen offset:0 lds\n"
    "ds_read_b64_tr_b16 a[224:225], v90 offset:192\n"
    "ds_read_b64_tr_b16 a[228:229], v91 offset:192\n"
    "ds_read_b64_tr_b16 a[232:233], v92 offset:192\n"
    "ds_read_b64_tr_b16 a[236:237], v93 offset:192\n"
    "s_add_u32 m0, s6, 122560\n"
    "s_waitcnt vmcnt(13)\n"
    "buffer_load_dwordx4 v83, s[28:31], s95 offen offset:2048 lds\n"
    "ds_read_b64_tr_b16 a[240:241], v90 offset:224\n"
    "ds_read_b64_tr_b16 a[244:245], v91 offset:224\n"
    "ds_read_b64_tr_b16 a[248:249], v92 offset:224\n"
    "ds_read_b64_tr_b16 a[252:253], v93 offset:224\n"
    "s_xor_b32 s95, s5, 4055040\n"
    "s_add_u32 m0, s6, 126720\n"
    "s_waitcnt vmcnt(13)\n"
    "buffer_load_dwordx4 v83, s[28:31], s95 offen offset:0 lds\n"
    "v_add_u32 v89, 67584, v89\n"
    "v_add_u32 v90, 67584, v90\n"
    "v_add_u32 v91, 67584, v91\n"
    "v_add_u32 v92, 67584, v92\n"
    "s_add_u32 m0, s6, 126784\n"
    "s_waitcnt vmcnt(13)\n"
    "buffer_load_dwordx4 v83, s[28:31], s95 offen offset:2048 lds\n"
    "v_add_u32 v93, 67584, v93\n"
    "s_waitcnt lgkmcnt(0)\n"
    "s_xor_b32 s95, s5, 4190208\n"
    "s_add_u32 m0, s6, 130944\n"
    "s_waitcnt vmcnt(13)\n"
    "buffer_load_dwordx4 v83, s[28:31], s95 offen offset:0 lds\n"
    "s_add_u32 m0, s6, 131008\n"
    "s_waitcnt vmcnt(13)\n"
    "buffer_load_dwordx4 v83, s[28:31], s95 offen offset:2048 lds\n"
    "s_branch LbA\n"
    "LwA:\n"
    "s_setprio 2\n"
    "s_barrier\n"
    "buffer_load_dwordx4 v[184:187], v158, s[20:23], 0 offen offset:0\n"
    "buffer_load_dwordx4 v[188:191], v158, s[20:23], 0 offen offset:256\n"
    "buffer_load_dwordx4 v[192:195], v158, s[20:23], 0 offen offset:512\n"
    "buffer_load_dwordx4 v[196:199], v158, s[20:23], 0 offen offset:768\n"
    "s_bfe_u32 s85, s61, 0x20001\n"
    "s_lshl_b32 s84, s85, 8\n"
    "buffer_load_dwordx4 v[160:163], v242, s[20:23], s84 offen\n"
    "v_mov_b32 v176, 0x3f803f80\n"
    "v_mov_b32 v177, 0x3f803f80\n"
    "v_mov_b32 v178, 0x3f803f80\n"
    "v_mov_b32 v179, 0x3f803f80\n"
    "s_mov_b32 s87, 0x26e00\n"
    "v_lshl_add_u32 v153, v1, 4, s87\n"
    "s_mov_b32 s87, 0x26e00\n"
    "v_mul_u32_u24 v230, 320, v10\n"
    "v_add_u32 v154, s87, v230\n"
    "s_mov_b32 s87, 0x27200\n"
    "v_lshl_add_u32 v155, v10, 6, s87\n"
    "s_waitcnt lgkmcnt(0)\n"
    "s_mov_b32 s11, s2\n"
    "s_mul_i32 s98, s59, 4224\n"
    "s_mul_i32 s99, s59, 16896\n"
    "s_and_b32 s65, s63, 1\n"
    "v_xor_b32 v228, s59, v9\n"
    "v_xor_b32 v229, s59, v10\n"
    "s_mov_b32 s87, 0x21000\n"
    "v_mul_u32_u24 v230, 4224, v3\n"
    "v_add_u32 v99, s87, v230\n"
    "v_lshl_add_u32 v99, v9, 7, v99\n"
    "v_lshl_add_u32 v99, v10, 3, v99\n"
    "v_lshl_add_u32 v99, v228, 5, v99\n"
    "s_mov_b32 s87, 0x1ce00\n"
    "v_mul_u32_u24 v230, 1056, v9\n"
    "v_add_u32 v104, s87, v230\n"
    "v_lshl_add_u32 v104, v10, 8, v104\n"
    "v_lshl_add_u32 v104, v3, 4, v104\n"
    "v_lshl_add_u32 v104, v229, 6, v104\n"
    "s_lshl_b32 s84, s59, 10\n"
    "s_add_u32 s87, s84, 0x25200\n"
    "v_lshl_add_u32 v127, v1, 4, s87\n"
    "s_add_u32 s84, s84, s68\n"
    "v_lshl_add_u32 v122, v1, 4, s84\n"
    "v_lshl_add_u32 v228, v3, 6, 0\n"
    "v_lshl_add_u32 v228, v10, 4, v228\n"
    "v_lshl_add_u32 v229, v9, 8, 0\n"
    "v_lshl_add_u32 v229, v228, 0, v229\n"
    "s_bfe_u32 s85, s69, 0x30001\n"
    "s_lshl_b32 s4, s85, 14\n"
    "s_lshl_b32 s86, s64, 12\n"
    "s_add_u32 s4, s4, s86\n"
    "s_add_u32 s4, s4, s68\n"
    "s_lshl_b32 s84, s59, 10\n"
    "s_add_u32 s84, s84, s4\n"
    "s_add_u32 s87, s84, 0x40000\n"
    "v_lshl_add_u32 v113, v229, 0, s87\n"
    "v_lshrrev_b32 v226, 1, v3\n"
    "v_and_b32 v227, 1, v3\n"
    "s_bfe_u32 s85, s61, 0x10000\n"
    "s_lshl_b32 s84, s85, 11\n"
    "s_bfe_u32 s85, s59, 0x10001\n"
    "s_lshl_b32 s86, s85, 10\n"
    "s_add_u32 s84, s84, s86\n"
    "s_bfe_u32 s85, s61, 0x20001\n"
    "s_lshl_b32 s86, s85, 8\n"
    "s_add_u32 s84, s84, s86\n"
    "s_bfe_u32 s85, s59, 0x10000\n"
    "s_lshl_b32 s86, s85, 7\n"
    "s_add_u32 s84, s84, s86\n"
    "s_add_u32 s84, s84, s4\n"
    "s_add_u32 s87, s84, 0x40000\n"
    "v_lshl_add_u32 v112, v226, 6, s87\n"
    "v_lshl_add_u32 v112, v10, 4, v112\n"
    "v_lshl_add_u32 v112, v227, 3, v112\n"
    "s_lshl_b32 s84, s59, 10\n"
    "s_lshl_b32 s86, s61, 7\n"
    "s_add_u32 s84, s84, s86\n"
    "s_add_u32 s84, s84, s4\n"
    "v_lshl_add_u32 v123, v228, 0, s84\n"
    "v_cmp_eq_u32 vcc, 0, v9\n"
    "s_nop 1\n"
    "v_cndmask_b32 v112, v239, v112, vcc\n"
    "v_cndmask_b32 v123, v239, v123, vcc\n"
    "s_bfe_u32 s85, s57, 0x20000\n"
    "s_lshl_b32 s84, s85, 15\n"
    "s_bfe_u32 s85, s59, 0x10001\n"
    "s_lshl_b32 s86, s85, 14\n"
    "s_add_u32 s84, s84, s86\n"
    "s_bfe_u32 s85, s59, 0x10000\n"
    "s_lshl_b32 s86, s85, 12\n"
    "s_add_u32 s84, s84, s86\n"
    "s_add_u32 s84, s84, s68\n"
    "v_lshl_add_u32 v114, v229, 0, s84\n"
    "s_bfe_u32 s85, s59, 0x10000\n"
    "s_lshl_b32 s84, s85, 16\n"
    "s_lshl_b32 s86, s64, 15\n"
    "s_add_u32 s84, s84, s86\n"
    "s_bfe_u32 s85, s60, 0x10004\n"
    "s_lshl_b32 s86, s85, 14\n"
    "s_add_u32 s84, s84, s86\n"
    "s_bfe_u32 s85, s60, 0x30001\n"
    "s_lshl_b32 s86, s85, 10\n"
    "s_add_u32 s84, s84, s86\n"
    "s_lshl_b32 s86, s61, 7\n"
    "s_add_u32 s84, s84, s86\n"
    "s_bfe_u32 s85, s59, 0x10001\n"
    "s_lshl_b32 s86, s85, 13\n"
    "s_add_u32 s84, s84, s86\n"
    "s_add_u32 s87, s84, 0x60000\n"
    "v_lshl_add_u32 v115, v1, 1, s87\n"
    "v_lshl_or_b32 v115, v1, 31, v115\n"
    "s_lshl_b32 s84, s64, 13\n"
    "v_xor_b32 v105, s84, v113\n"
    "v_xor_b32 v106, s84, v112\n"
    "s_mov_b32 s92, 0xb0000\n"
    "v_xor_b32 v230, 1, v9\n"
    "v_sub_u32 v230, v230, v9\n"
    "v_lshl_add_u32 v48, v230, 5, v99\n"
    "v_xor_b32 v230, 1, v10\n"
    "v_sub_u32 v230, v230, v10\n"
    "v_lshl_add_u32 v51, v230, 6, v104\n"
    "v_xor_b32 v230, 2, v9\n"
    "v_sub_u32 v230, v230, v9\n"
    "v_lshl_add_u32 v49, v230, 5, v99\n"
    "v_xor_b32 v230, 2, v10\n"
    "v_sub_u32 v230, v230, v10\n"
    "v_lshl_add_u32 v52, v230, 6, v104\n"
    "v_xor_b32 v230, 3, v9\n"
    "v_sub_u32 v230, v230, v9\n"
    "v_lshl_add_u32 v50, v230, 5, v99\n"
    "v_xor_b32 v230, 3, v10\n"
    "v_sub_u32 v230, v230, v10\n"
    "v_lshl_add_u32 v53, v230, 6, v104\n"
    "s_bfe_u32 s4, s60, 0x20003\n"
    "s_xor_b32 s4, s4, s59\n"
    "s_lshl_b32 s84, s69, 18\n"
    "s_lshl_b32 s86, s61, 15\n"
    "s_add_u32 s84, s84, s86\n"
    "s_bfe_u32 s85, s4, 0x10001\n"
    "s_lshl_b32 s86, s85, 14\n"
    "s_add_u32 s84, s84, s86\n"
    "s_bfe_u32 s85, s4, 0x10000\n"
    "s_lshl_b32 s86, s85, 12\n"
    "s_add_u32 s84, s84, s86\n"
    "s_bfe_u32 s85, s60, 0x10001\n"
    "s_lshl_b32 s4, s85, 5\n"
    "s_bfe_u32 s85, s69, 0x10003\n"
    "s_lshl_b32 s85, s85, 13\n"
    "s_xor_b32 s85, s85, s68\n"
    "s_add_u32 s84, s84, s85\n"
    "v_lshlrev_b32 v224, 1, v3\n"
    "v_xor_b32 v224, v224, v1\n"
    "v_xor_b32 v224, s4, v224\n"
    "s_add_u32 s87, s84, 0x400000\n"
    "v_lshl_add_u32 v118, v224, 4, s87\n"
    "s_lshl_b32 s84, s59, 6\n"
    "v_and_b32 v225, 1, v3\n"
    "v_lshrrev_b32 v226, 1, v9\n"
    "v_and_b32 v227, 7, v2\n"
    "s_add_u32 s87, s84, 0x26300\n"
    "v_lshl_add_u32 v135, v9, 4, s87\n"
    "v_lshl_add_u32 v135, v225, 3, v135\n"
    "v_lshl_add_u32 v135, v10, 1, v135\n"
    "s_add_u32 s87, s84, 0x26300\n"
    "v_lshl_add_u32 v136, v3, 4, s87\n"
    "s_mov_b32 s87, 0x21000\n"
    "v_mul_u32_u24 v230, 1056, v2\n"
    "v_add_u32 v124, s87, v230\n"
    "v_lshl_add_u32 v124, v3, 4, v124\n"
    "s_lshl_b32 s84, s59, 2\n"
    "s_add_u32 s87, s84, 0x26200\n"
    "v_lshl_add_u32 v125, v2, 4, s87\n"
    "s_mov_b32 s87, 0x26200\n"
    "v_lshl_add_u32 v126, v2, 4, s87\n"
    "s_add_u32 s84, s59, 1\n"
    "s_and_b32 s84, s84, 3\n"
    "s_lshl_b32 s84, s84, 10\n"
    "s_add_u32 s87, s84, 0x25200\n"
    "v_lshl_add_u32 v128, v1, 4, s87\n"
    "s_add_u32 s84, s59, 2\n"
    "s_and_b32 s84, s84, 3\n"
    "s_lshl_b32 s84, s84, 10\n"
    "s_add_u32 s87, s84, 0x25200\n"
    "v_lshl_add_u32 v129, v1, 4, s87\n"
    "s_add_u32 s84, s59, 3\n"
    "s_and_b32 s84, s84, 3\n"
    "s_lshl_b32 s84, s84, 10\n"
    "s_add_u32 s87, s84, 0x25200\n"
    "v_lshl_add_u32 v130, v1, 4, s87\n"
    "s_lshl_b32 s84, s59, 13\n"
    "s_add_u32 s87, s84, 0x10800\n"
    "v_lshl_add_u32 v131, v3, 9, s87\n"
    "v_lshl_add_u32 v131, v9, 7, v131\n"
    "v_lshl_add_u32 v131, v225, 6, v131\n"
    "v_lshl_add_u32 v131, v226, 5, v131\n"
    "v_lshl_add_u32 v131, v10, 3, v131\n"
    "v_xor_b32 v132, 32, v131\n"
    "v_xor_b32 v133, 64, v131\n"
    "v_xor_b32 v134, 96, v131\n"
    "s_lshl_b32 s84, s59, 4\n"
    "s_add_u32 s87, s84, 0x26a00\n"
    "v_lshl_add_u32 v137, v3, 6, s87\n"
    "s_add_u32 s87, s84, 0x26440\n"
    "v_mov_b32 v143, s87\n"
    "s_mov_b32 s87, 0x26a00\n"
    "v_lshl_add_u32 v138, v3, 6, s87\n"
    "v_lshl_add_u32 v138, v10, 4, v138\n"
    "s_lshl_b32 s84, s59, 8\n"
    "s_add_u32 s87, s84, 0x25200\n"
    "v_lshl_add_u32 v141, v1, 2, s87\n"
    "s_lshl_b32 s84, s59, 10\n"
    "s_add_u32 s87, s84, 0x25200\n"
    "v_lshl_add_u32 v139, v1, 2, s87\n"
    "s_mov_b32 s87, 0x25200\n"
    "v_lshl_add_u32 v140, v1, 2, s87\n"
    "s_lshl_b32 s84, s59, 2\n"
    "v_lshrrev_b32 v224, 1, v3\n"
    "s_add_u32 s87, s84, 0x26440\n"
    "v_lshl_add_u32 v142, v224, 6, s87\n"
    "v_lshl_add_u32 v142, v10, 4, v142\n"
    "v_and_b32 v224, 1, v1\n"
    "v_lshl_add_u32 v121, v226, 6, 0\n"
    "v_lshl_add_u32 v121, v3, 4, v121\n"
    "v_lshl_add_u32 v121, v227, 1, v121\n"
    "v_mul_u32_u24 v230, 8190, v224\n"
    "v_add_u32 v121, v121, v230\n"
    "s_bfe_u32 s85, s58, 0x30002\n"
    "s_lshl_b32 s97, s85, 1\n"
    "s_add_u32 s97, s97, s64\n"
    "s_bfe_u32 s85, s62, 0x10000\n"
    "s_lshl_b32 s84, s85, 16\n"
    "s_lshl_b32 s86, s64, 15\n"
    "s_add_u32 s84, s84, s86\n"
    "s_waitcnt vmcnt(4)\n"
    "v_mfma_f32_16x16x32_bf16 v[200:203], v[184:187], v[184:187], 0\n"
    "v_mfma_f32_16x16x32_bf16 v[204:207], v[176:179], v[184:187], 0\n"
    "s_waitcnt vmcnt(3)\n"
    "v_mfma_f32_16x16x32_bf16 v[200:203], v[188:191], v[188:191], v[200:203]\n"
    "v_mfma_f32_16x16x32_bf16 v[204:207], v[176:179], v[188:191], v[204:207]\n"
    "s_waitcnt vmcnt(2)\n"
    "v_mfma_f32_16x16x32_bf16 v[200:203], v[192:195], v[192:195], v[200:203]\n"
    "v_mfma_f32_16x16x32_bf16 v[204:207], v[176:179], v[192:195], v[204:207]\n"
    "s_waitcnt vmcnt(1)\n"
    "v_mfma_f32_16x16x32_bf16 v[200:203], v[196:199], v[196:199], v[200:203]\n"
    "v_mfma_f32_16x16x32_bf16 v[204:207], v[176:179], v[196:199], v[204:207]\n"
    "s_nop 6\n"
    "ds_write_b128 v153, v[200:203]\n"
    "ds_write_b128 v153, v[204:207] offset:1024\n"
    "ds_read2_b32 v[208:209], v154 offset0:0 offset1:5\n"
    "ds_read2_b32 v[210:211], v154 offset0:10 offset1:15\n"
    "ds_read2_b32 v[212:213], v155 offset0:0 offset1:4\n"
    "ds_read2_b32 v[214:215], v155 offset0:8 offset1:12\n"
    "s_waitcnt vmcnt(0)\n"
    "v_lshlrev_b32 v216, 16, v160\n"
    "v_and_b32 v217, 0xffff0000, v160\n"
    "v_lshlrev_b32 v218, 16, v161\n"
    "v_and_b32 v219, 0xffff0000, v161\n"
    "v_lshlrev_b32 v220, 16, v162\n"
    "v_and_b32 v221, 0xffff0000, v162\n"
    "v_lshlrev_b32 v222, 16, v163\n"
    "v_and_b32 v223, 0xffff0000, v163\n"
    "s_waitcnt lgkmcnt(2)\n"
    "v_pk_add_f32 v[208:209], v[208:209], v[210:211]\n"
    "v_add_f32 v208, v208, v209\n"
    "s_waitcnt lgkmcnt(0)\n"
    "v_pk_add_f32 v[212:213], v[212:213], v[214:215]\n"
    "v_add_f32 v212, v212, v213\n"
    "v_mul_f32 v6, 0x3b000000, v212\n"
    "v_mul_f32 v206, 0x3b000000, v208\n"
    "v_fma_f32 v206, -v6, v6, v206\n"
    "v_max_f32 v206, 0, v206\n"
    "v_add_f32 v206, 0x3727c5ac, v206\n"
    "v_rsq_f32 v5, v206\n"
    "s_nop 0\n"
    "v_mul_f32 v6, v6, v5\n"
    "v_fma_f32 v216, v216, v5, -v6\n"
    "v_fma_f32 v217, v217, v5, -v6\n"
    "v_fma_f32 v218, v218, v5, -v6\n"
    "v_fma_f32 v219, v219, v5, -v6\n"
    "v_fma_f32 v220, v220, v5, -v6\n"
    "v_fma_f32 v221, v221, v5, -v6\n"
    "v_fma_f32 v222, v222, v5, -v6\n"
    "v_fma_f32 v223, v223, v5, -v6\n"
    "v_cvt_pk_bf16_f32 v160, v216, v217\n"
    "v_cvt_pk_bf16_f32 v161, v218, v219\n"
    "v_cvt_pk_bf16_f32 v162, v220, v221\n"
    "v_cvt_pk_bf16_f32 v163, v222, v223\n"
    "v_mov_b32_dpp v164, v160 row_ror:12 row_mask:0xf bank_mask:0xf\n"
    "v_mov_b32_dpp v165, v161 row_ror:12 row_mask:0xf bank_mask:0xf\n"
    "v_mov_b32_dpp v166, v162 row_ror:12 row_mask:0xf bank_mask:0xf\n"
    "v_mov_b32_dpp v167, v163 row_ror:12 row_mask:0xf bank_mask:0xf\n"
    "v_mov_b32_dpp v168, v160 row_ror:8 row_mask:0xf bank_mask:0xf\n"
    "v_mov_b32_dpp v169, v161 row_ror:8 row_mask:0xf bank_mask:0xf\n"
    "v_mov_b32_dpp v170, v162 row_ror:8 row_mask:0xf bank_mask:0xf\n"
    "v_mov_b32_dpp v171, v163 row_ror:8 row_mask:0xf bank_mask:0xf\n"
    "v_mov_b32_dpp v172, v160 row_ror:4 row_mask:0xf bank_mask:0xf\n"
    "v_mov_b32_dpp v173, v161 row_ror:4 row_mask:0xf bank_mask:0xf\n"
    "v_mov_b32_dpp v174, v162 row_ror:4 row_mask:0xf bank_mask:0xf\n"
    "v_mov_b32_dpp v175, v163 row_ror:4 row_mask:0xf bank_mask:0xf\n"
    "s_barrier\n"
    "ds_read_b64_tr_b16 a[0:1], v99 offset:0\n"
    "ds_read_b64_tr_b16 a[2:3], v99 offset:512\n"
    "ds_read_b64_tr_b16 a[16:17], v48 offset:0\n"
    "ds_read_b64_tr_b16 a[18:19], v48 offset:512\n"
    "ds_read_b64_tr_b16 a[32:33], v49 offset:0\n"
    "ds_read_b64_tr_b16 a[34:35], v49 offset:512\n"
    "ds_read_b64_tr_b16 a[48:49], v50 offset:0\n"
    "ds_read_b64_tr_b16 a[50:51], v50 offset:512\n"
    "ds_read_b64_tr_b16 a[4:5], v99 offset:1056\n"
    "ds_read_b64_tr_b16 a[6:7], v99 offset:1568\n"
    "ds_read_b64_tr_b16 a[20:21], v48 offset:1056\n"
    "ds_read_b64_tr_b16 a[22:23], v48 offset:1568\n"
    "ds_read_b64_tr_b16 a[36:37], v49 offset:1056\n"
    "ds_read_b64_tr_b16 a[38:39], v49 offset:1568\n"
    "ds_read_b64_tr_b16 a[52:53], v50 offset:1056\n"
    "ds_read_b64_tr_b16 a[54:55], v50 offset:1568\n"
    "s_waitcnt lgkmcnt(14)\n"
    "v_mfma_f32_16x16x32_bf16 v[176:179], a[0:3], v[160:163], 0\n"
    "s_waitcnt lgkmcnt(12)\n"
    "v_mfma_f32_16x16x32_bf16 v[180:183], a[16:19], v[160:163], 0\n"
    "s_waitcnt lgkmcnt(10)\n"
    "v_mfma_f32_16x16x32_bf16 v[184:187], a[32:35], v[160:163], 0\n"
    "s_waitcnt lgkmcnt(8)\n"
    "v_mfma_f32_16x16x32_bf16 v[188:191], a[48:51], v[160:163], 0\n"
    "s_waitcnt lgkmcnt(6)\n"
    "v_mfma_f32_16x16x32_bf16 v[176:179], a[4:7], v[164:167], v[176:179]\n"
    "s_waitcnt lgkmcnt(4)\n"
    "v_mfma_f32_16x16x32_bf16 v[180:183], a[20:23], v[164:167], v[180:183]\n"
    "s_waitcnt lgkmcnt(2)\n"
    "v_mfma_f32_16x16x32_bf16 v[184:187], a[36:39], v[164:167], v[184:187]\n"
    "s_waitcnt lgkmcnt(0)\n"
    "v_mfma_f32_16x16x32_bf16 v[188:191], a[52:55], v[164:167], v[188:191]\n"
    "s_barrier\n"
    "ds_read_b64_tr_b16 a[8:9], v99 offset:2112\n"
    "ds_read_b64_tr_b16 a[10:11], v99 offset:2624\n"
    "ds_read_b64_tr_b16 a[24:25], v48 offset:2112\n"
    "ds_read_b64_tr_b16 a[26:27], v48 offset:2624\n"
    "ds_read_b64_tr_b16 a[40:41], v49 offset:2112\n"
    "ds_read_b64_tr_b16 a[42:43], v49 offset:2624\n"
    "ds_read_b64_tr_b16 a[56:57], v50 offset:2112\n"
    "ds_read_b64_tr_b16 a[58:59], v50 offset:2624\n"
    "ds_read_b64_tr_b16 a[12:13], v99 offset:3168\n"
    "ds_read_b64_tr_b16 a[14:15], v99 offset:3680\n"
    "ds_read_b64_tr_b16 a[28:29], v48 offset:3168\n"
    "ds_read_b64_tr_b16 a[30:31], v48 offset:3680\n"
    "ds_read_b64_tr_b16 a[44:45], v49 offset:3168\n"
    "ds_read_b64_tr_b16 a[46:47], v49 offset:3680\n"
    "ds_read_b64_tr_b16 a[60:61], v50 offset:3168\n"
    "ds_read_b64_tr_b16 a[62:63], v50 offset:3680\n"
    "s_waitcnt lgkmcnt(14)\n"
    "v_mfma_f32_16x16x32_bf16 v[176:179], a[8:11], v[168:171], v[176:179]\n"
    "s_waitcnt lgkmcnt(12)\n"
    "v_mfma_f32_16x16x32_bf16 v[180:183], a[24:27], v[168:171], v[180:183]\n"
    "s_waitcnt lgkmcnt(10)\n"
    "v_mfma_f32_16x16x32_bf16 v[184:187], a[40:43], v[168:171], v[184:187]\n"
    "s_waitcnt lgkmcnt(8)\n"
    "v_mfma_f32_16x16x32_bf16 v[188:191], a[56:59], v[168:171], v[188:191]\n"
    "s_waitcnt lgkmcnt(6)\n"
    "v_mfma_f32_16x16x32_bf16 v[176:179], a[12:15], v[172:175], v[176:179]\n"
    "s_waitcnt lgkmcnt(4)\n"
    "v_mfma_f32_16x16x32_bf16 v[180:183], a[28:31], v[172:175], v[180:183]\n"
    "s_waitcnt lgkmcnt(2)\n"
    "v_mfma_f32_16x16x32_bf16 v[184:187], a[44:47], v[172:175], v[184:187]\n"
    "s_waitcnt lgkmcnt(0)\n"
    "v_mfma_f32_16x16x32_bf16 v[188:191], a[60:63], v[172:175], v[188:191]\n"
    "s_nop 1\n"
    "v_cvt_pk_bf16_f32 v192, v176, v177\n"
    "v_cvt_pk_bf16_f32 v193, v178, v179\n"
    "buffer_store_dwordx2 v[192:193], v106, s[12:15], s92 offen offset:0 sc1\n"
    "v_cvt_pk_bf16_f32 v194, v180, v181\n"
    "v_cvt_pk_bf16_f32 v195, v182, v183\n"
    "buffer_store_dwordx2 v[194:195], v106, s[12:15], s92 offen offset:128 sc1\n"
    "v_cvt_pk_bf16_f32 v196, v184, v185\n"
    "v_cvt_pk_bf16_f32 v197, v186, v187\n"
    "buffer_store_dwordx2 v[196:197], v106, s[12:15], s92 offen offset:1024 sc1\n"
    "v_cvt_pk_bf16_f32 v198, v188, v189\n"
    "v_cvt_pk_bf16_f32 v199, v190, v191\n"
    "buffer_store_dwordx2 v[198:199], v106, s[12:15], s92 offen offset:1152 sc1\n"
    "s_bfe_u32 s85, s59, 0x10001\n"
    "s_lshl_b32 s86, s85, 14\n"
    "s_add_u32 s84, s84, s86\n"
    "s_bfe_u32 s85, s59, 0x10000\n"
    "s_lshl_b32 s86, s85, 12\n"
    "s_add_u32 s84, s84, s86\n"
    "s_add_u32 s84, s84, s68\n"
    "s_add_u32 s87, s84, 0x60000\n"
    "v_lshl_add_u32 v116, v1, 4, s87\n"
    "s_lshl_b32 s84, s64, 18\n"
    "s_lshl_b32 s86, s59, 16\n"
    "s_add_u32 s84, s84, s86\n"
    "v_lshrrev_b32 v224, 1, v3\n"
    "v_xor_b32 v224, v224, v225\n"
    "v_xor_b32 v228, s62, v3\n"
    "v_and_b32 v229, 1, v228\n"
    "v_lshrrev_b32 v228, 1, v228\n"
    "s_add_u32 s87, s84, 0x400000\n"
    "v_lshl_add_u32 v117, v225, 21, s87\n"
    "v_lshl_add_u32 v117, v224, 13, v117\n"
    "v_lshl_add_u32 v117, v2, 3, v117\n"
    "v_lshl_add_u32 v117, v229, 12, v117\n"
    "v_lshl_add_u32 v117, v228, 14, v117\n"
    "s_lshl_b32 s4, s59, 3\n"
    "s_bfe_u32 s85, s63, 0x40001\n"
    "s_xor_b32 s4, s4, s85\n"
    "s_lshl_b32 s84, s65, 20\n"
    "s_lshl_b32 s86, s4, 7\n"
    "s_add_u32 s84, s84, s86\n"
    "s_xor_b32 s70, s84, 0x0\n"
    "s_xor_b32 s71, s84, 0x80200\n"
    "s_xor_b32 s72, s84, 0x100800\n"
    "s_xor_b32 s73, s84, 0x180a00\n"
    "s_xor_b32 s74, s84, 0x8000\n"
    "s_xor_b32 s75, s84, 0x88200\n"
    "s_xor_b32 s76, s84, 0x108800\n"
    "s_xor_b32 s77, s84, 0x188a00\n"
    "s_lshl_b32 s84, s62, 14\n"
    "s_lshl_b32 s86, s64, 12\n"
    "s_add_u32 s84, s84, s86\n"
    "s_lshl_b32 s86, s63, 7\n"
    "s_add_u32 s84, s84, s86\n"
    "s_cmp_eq_u32 s59, 0\n"
    "s_cselect_b32 s84, s84, 0x80000000\n"
    "s_lshl_b32 s85, s65, 1\n"
    "v_xor_b32 v224, s85, v2\n"
    "s_add_u32 s87, s84, 0x80000\n"
    "v_lshl_add_u32 v119, v224, 3, s87\n"
    "v_lshl_add_u32 v119, v3, 13, v119\n"
    "v_cmp_gt_u32 vcc, 2, v3\n"
    "s_nop 1\n"
    "v_cndmask_b32 v119, v239, v119, vcc\n"
    "s_barrier\n"
    "ds_read_b128 a[64:67], v104 offset:0\n"
    "ds_read_b128 a[68:71], v104 offset:4224\n"
    "ds_read_b128 a[72:75], v104 offset:8448\n"
    "ds_read_b128 a[76:79], v104 offset:12672\n"
    "ds_read_b128 a[80:83], v51 offset:0\n"
    "ds_read_b128 a[84:87], v51 offset:4224\n"
    "ds_read_b128 a[88:91], v51 offset:8448\n"
    "ds_read_b128 a[92:95], v51 offset:12672\n"
    "ds_read_b128 a[96:99], v52 offset:0\n"
    "ds_read_b128 a[100:103], v52 offset:4224\n"
    "ds_read_b128 a[104:107], v52 offset:8448\n"
    "ds_read_b128 a[108:111], v52 offset:12672\n"
    "ds_read_b128 a[112:115], v53 offset:0\n"
    "ds_read_b128 a[116:119], v53 offset:4224\n"
    "ds_read_b128 a[120:123], v53 offset:8448\n"
    "ds_read_b128 a[124:127], v53 offset:12672\n"
    "ds_read_b64_tr_b16 v[16:17], v99 offset:0\n"
    "ds_read_b64_tr_b16 v[18:19], v99 offset:512\n"
    "ds_read_b64_tr_b16 v[20:21], v99 offset:1056\n"
    "ds_read_b64_tr_b16 v[22:23], v99 offset:1568\n"
    "ds_read_b64_tr_b16 v[24:25], v99 offset:2112\n"
    "ds_read_b64_tr_b16 v[26:27], v99 offset:2624\n"
    "ds_read_b64_tr_b16 v[28:29], v99 offset:3168\n"
    "ds_read_b64_tr_b16 v[30:31], v99 offset:3680\n"
    "ds_read_b128 v[32:35], v104 offset:0\n"
    "ds_read_b128 v[36:39], v104 offset:4224\n"
    "ds_read_b128 v[40:43], v104 offset:8448\n"
    "ds_read_b128 v[44:47], v104 offset:12672\n"
    "s_waitcnt lgkmcnt(0)\n"
    "s_barrier\n"
    "Lgw:\n"
    "ds_read_b32 v204, v157 offset:16\n"
    "s_waitcnt lgkmcnt(0)\n"
    "v_readfirstlane_b32 s78, v204\n"
    "s_cmp_eq_u32 s78, -1\n"
    "s_cbranch_scc1 Lgw\n"
    "Lwt_13:\n"
    "s_memrealtime s[78:79]\n"
    "s_waitcnt lgkmcnt(0)\n"
    "s_sub_u32 s78, s78, s11\n"
    "s_cmp_lt_u32 s78, 240\n"
    "s_cbranch_scc1 Lwt_13\n"
    "buffer_load_dwordx4 v[144:147], v105, s[12:15], s92 offen offset:0 sc1\n"
    "buffer_load_dwordx4 v[148:151], v105, s[12:15], s92 offen offset:1024 sc1\n"
    "buffer_load_dwordx4 v[152:155], v105, s[12:15], s92 offen offset:2048 sc1\n"
    "buffer_load_dwordx4 v[156:159], v105, s[12:15], s92 offen offset:3072 sc1\n"
    "s_bfe_u32 s4, s60, 0x10002\n"
    "s_lshl_b32 s84, s59, 14\n"
    "s_add_u32 s84, s84, s68\n"
    "s_lshl_b32 s86, s64, 12\n"
    "s_add_u32 s84, s84, s86\n"
    "s_bfe_u32 s85, s60, 0x40001\n"
    "s_lshl_b32 s86, s85, 3\n"
    "s_add_u32 s84, s84, s86\n"
    "v_xor_b32 v224, s4, v225\n"
    "s_add_u32 s87, s84, 0x80000\n"
    "v_lshl_add_u32 v120, v2, 8, s87\n"
    "v_lshl_add_u32 v120, v224, 7, v120\n"
    "s_mov_b32 s83, 0xb0000\n"
    "s_mul_i32 s84, s56, 1536\n"
    "s_add_u32 s87, s84, 0x90000\n"
    "v_lshl_add_u32 v80, v0, 4, s87\n"
    "v_cmp_gt_u32 vcc, 96, v0\n"
    "s_nop 1\n"
    "v_cndmask_b32 v80, v239, v80, vcc\n"
    "v_lshrrev_b32 v228, 1, v1\n"
    "v_and_b32 v229, 1, v1\n"
    "s_lshl_b32 s84, s58, 7\n"
    "s_lshl_b32 s86, s57, 2\n"
    "s_add_u32 s84, s84, s86\n"
    "s_add_u32 s87, s84, 0x140000\n"
    "v_lshl_add_u32 v88, v0, 13, s87\n"
    "v_cmp_gt_u32 vcc, 2, v0\n"
    "s_nop 1\n"
    "v_cndmask_b32 v88, v239, v88, vcc\n"
    "s_add_u32 s87, s68, 0x140000\n"
    "v_lshl_add_u32 v236, v228, 7, s87\n"
    "v_lshl_add_u32 v236, v229, 4, v236\n"
    "v_mul_u32_u24 v230, 1056, v2\n"
    "v_add_u32 v89, s99, v230\n"
    "v_lshl_add_u32 v89, v3, 4, v89\n"
    "v_mul_u32_u24 v230, 4224, v3\n"
    "v_add_u32 v229, 0, v230\n"
    "v_mul_u32_u24 v230, 1056, v9\n"
    "v_add_u32 v229, v229, v230\n"
    "v_lshl_add_u32 v229, v10, 3, v229\n"
    "s_add_u32 s84, s59, 0\n"
    "s_and_b32 s84, s84, 3\n"
    "s_mul_i32 s84, s84, 16896\n"
    "s_lshl_b32 s86, s59, 8\n"
    "s_add_u32 s84, s84, s86\n"
    "v_add_u32 v90, s84, v229\n"
    "s_add_u32 s84, s59, 1\n"
    "s_and_b32 s84, s84, 3\n"
    "s_mul_i32 s84, s84, 16896\n"
    "s_lshl_b32 s86, s59, 8\n"
    "s_add_u32 s84, s84, s86\n"
    "v_add_u32 v91, s84, v229\n"
    "s_add_u32 s84, s59, 2\n"
    "s_and_b32 s84, s84, 3\n"
    "s_mul_i32 s84, s84, 16896\n"
    "s_lshl_b32 s86, s59, 8\n"
    "s_add_u32 s84, s84, s86\n"
    "v_add_u32 v92, s84, v229\n"
    "s_add_u32 s84, s59, 3\n"
    "s_and_b32 s84, s84, 3\n"
    "s_mul_i32 s84, s84, 16896\n"
    "s_lshl_b32 s86, s59, 8\n"
    "s_add_u32 s84, s84, s86\n"
    "v_add_u32 v93, s84, v229\n"
    "v_lshrrev_b32 v228, 1, v0\n"
    "s_lshl_b32 s84, s57, 19\n"
    "s_lshl_b32 s86, s97, 15\n"
    "s_add_u32 s84, s84, s86\n"
    "v_lshl_add_u32 v244, v228, 7, s84\n"
    "v_add_u32 v245, 0x4000, v244\n"
    "s_mul_i32 s84, s57, 7\n"
    "s_lshr_b32 s84, s84, 1\n"
    "s_lshl_b32 s84, s84, 7\n"
    "s_mul_i32 s86, s97, 114688\n"
    "s_add_u32 s84, s84, s86\n"
    "v_lshrrev_b32 v229, 3, v0\n"
    "v_and_b32 v227, 3, v228\n"
    "v_mul_u32_u24 v230, 3584, v229\n"
    "v_add_u32 v246, s84, v230\n"
    "v_lshl_add_u32 v246, v227, 7, v246\n"
    "v_min_u32 v228, 111, v228\n"
    "s_lshl_b32 s84, s57, 7\n"
    "s_mul_i32 s86, s97, 114688\n"
    "s_add_u32 s84, s84, s86\n"
    "v_lshl_add_u32 v247, v228, 10, s84\n"
    "s_lshl_b32 s84, s57, 19\n"
    "s_lshl_b32 s86, s97, 15\n"
    "s_add_u32 s84, s84, s86\n"
    "s_lshl_b32 s86, s59, 12\n"
    "s_add_u32 s84, s84, s86\n"
    "v_lshl_add_u32 v228, v1, 4, s84\n"
    "v_mov_b32 v229, s61\n"
    "v_and_b32 v229, 1, v229\n"
    "v_cmp_eq_u32 vcc, 0, v229\n"
    "s_nop 1\n"
    "v_cndmask_b32 v82, v228, v82, vcc\n"
    "s_mov_b32 s93, 0xb0000\n"
    "s_mov_b32 s94, 0xc0000\n"
    "v_xor_b32 v179, 0x2000, v115\n"
    "Lhq0c_14:\n"
    "s_waitcnt vmcnt(3)\n"
    "v_mfma_f32_16x16x32_bf16 v[208:211], v[144:147], a[64:67], 0\n"
    "v_mfma_f32_16x16x32_bf16 v[212:215], v[144:147], a[68:71], 0\n"
    "v_mfma_f32_16x16x32_bf16 v[216:219], v[144:147], a[72:75], 0\n"
    "v_mfma_f32_16x16x32_bf16 v[220:223], v[144:147], a[76:79], 0\n"
    "s_waitcnt vmcnt(2)\n"
    "v_mfma_f32_16x16x32_bf16 v[208:211], v[148:151], a[80:83], v[208:211]\n"
    "v_mfma_f32_16x16x32_bf16 v[212:215], v[148:151], a[84:87], v[212:215]\n"
    "v_mfma_f32_16x16x32_bf16 v[216:219], v[148:151], a[88:91], v[216:219]\n"
    "v_mfma_f32_16x16x32_bf16 v[220:223], v[148:151], a[92:95], v[220:223]\n"
    "s_waitcnt vmcnt(1)\n"
    "v_mfma_f32_16x16x32_bf16 v[208:211], v[152:155], a[96:99], v[208:211]\n"
    "v_mfma_f32_16x16x32_bf16 v[212:215], v[152:155], a[100:103], v[212:215]\n"
    "v_mfma_f32_16x16x32_bf16 v[216:219], v[152:155], a[104:107], v[216:219]\n"
    "v_mfma_f32_16x16x32_bf16 v[220:223], v[152:155], a[108:111], v[220:223]\n"
    "s_waitcnt vmcnt(0)\n"
    "v_mfma_f32_16x16x32_bf16 v[208:211], v[156:159], a[112:115], v[208:211]\n"
    "v_mfma_f32_16x16x32_bf16 v[212:215], v[156:159], a[116:119], v[212:215]\n"
    "v_mfma_f32_16x16x32_bf16 v[216:219], v[156:159], a[120:123], v[216:219]\n"
    "v_mfma_f32_16x16x32_bf16 v[220:223], v[156:159], a[124:127], v[220:223]\n"
    "s_nop 6\n"
    "v_permlane32_swap_b32 v208, v216\n"
    "v_permlane32_swap_b32 v212, v220\n"
    "v_permlane32_swap_b32 v209, v217\n"
    "v_permlane32_swap_b32 v213, v221\n"
    "v_permlane32_swap_b32 v210, v218\n"
    "v_permlane32_swap_b32 v214, v222\n"
    "v_permlane32_swap_b32 v211, v219\n"
    "v_permlane32_swap_b32 v215, v223\n"
    "v_add_f32 v208, v208, v216\n"
    "v_add_f32 v212, v212, v220\n"
    "v_add_f32 v209, v209, v217\n"
    "v_add_f32 v213, v213, v221\n"
    "v_add_f32 v210, v210, v218\n"
    "v_add_f32 v214, v214, v222\n"
    "v_add_f32 v211, v211, v219\n"
    "v_add_f32 v215, v215, v223\n"
    "v_permlane16_swap_b32 v208, v212\n"
    "v_permlane16_swap_b32 v209, v213\n"
    "v_permlane16_swap_b32 v210, v214\n"
    "v_permlane16_swap_b32 v211, v215\n"
    "v_add_f32 v208, v208, v212\n"
    "v_add_f32 v209, v209, v213\n"
    "v_add_f32 v210, v210, v214\n"
    "v_add_f32 v211, v211, v215\n"
    "v_add_f32 v220, v208, v209\n"
    "v_add_f32 v221, v210, v211\n"
    "v_cmp_u_f32 vcc, v220, v221\n"
    "v_mul_f32 v212, 0x3e0293ee, v208\n"
    "v_mul_f32 v214, 0x3e0293ee, v209\n"
    "v_mul_f32 v216, 0x3e0293ee, v210\n"
    "v_mul_f32 v218, 0x3e0293ee, v211\n"
    "s_cbranch_vccnz Lhq0s_15\n"
    "Lhq0d_16:\n"
    "v_mov_b32_dpp v213, v212 quad_perm:[1,1,3,3] row_mask:0xf bank_mask:0xf\n"
    "v_cvt_pk_bf16_f32 v212, v212, v213\n"
    "buffer_store_dword v212, v115, s[12:15], s93 offen sc1\n"
    "v_mov_b32_dpp v215, v214 quad_perm:[1,1,3,3] row_mask:0xf bank_mask:0xf\n"
    "v_cvt_pk_bf16_f32 v214, v214, v215\n"
    "buffer_store_dword v214, v115, s[12:15], s94 offen sc1\n"
    "v_mov_b32_dpp v217, v216 quad_perm:[1,1,3,3] row_mask:0xf bank_mask:0xf\n"
    "v_cvt_pk_bf16_f32 v216, v216, v217\n"
    "buffer_store_dword v216, v179, s[12:15], s93 offen sc1\n"
    "v_mov_b32_dpp v219, v218 quad_perm:[1,1,3,3] row_mask:0xf bank_mask:0xf\n"
    "v_cvt_pk_bf16_f32 v218, v218, v219\n"
    "buffer_store_dword v218, v179, s[12:15], s94 offen sc1\n"
    "s_setprio 0\n"
    "v_mov_b32 v226, 156672\n"
    "Lt0w2_17:\n"
    "ds_read_b64 v[224:225], v226 offset:24\n"
    "s_waitcnt lgkmcnt(0)\n"
    "v_or_b32 v224, v224, v225\n"
    "v_cmp_gt_i32 vcc, 0, v224\n"
    "s_cbranch_vccnz Lt0w2_17\n"
    "ds_read_b128 a[0:3], v89 offset:0\n"
    "ds_read_b128 a[4:7], v89 offset:64\n"
    "ds_read_b128 a[8:11], v89 offset:128\n"
    "ds_read_b128 a[12:15], v89 offset:192\n"
    "ds_read_b128 a[16:19], v89 offset:256\n"
    "ds_read_b128 a[20:23], v89 offset:320\n"
    "ds_read_b128 a[24:27], v89 offset:384\n"
    "ds_read_b128 a[28:31], v89 offset:448\n"
    "ds_read_b128 a[32:35], v89 offset:512\n"
    "ds_read_b128 a[36:39], v89 offset:576\n"
    "ds_read_b128 a[40:43], v89 offset:640\n"
    "ds_read_b128 a[44:47], v89 offset:704\n"
    "ds_read_b128 a[48:51], v89 offset:768\n"
    "ds_read_b128 a[52:55], v89 offset:832\n"
    "ds_read_b128 a[56:59], v89 offset:896\n"
    "ds_read_b128 a[60:63], v89 offset:960\n"
    "ds_read_b64_tr_b16 a[128:129], v90 offset:0\n"
    "ds_read_b64_tr_b16 a[132:133], v91 offset:0\n"
    "ds_read_b64_tr_b16 a[136:137], v92 offset:0\n"
    "ds_read_b64_tr_b16 a[140:141], v93 offset:0\n"
    "ds_read_b64_tr_b16 a[144:145], v90 offset:32\n"
    "ds_read_b64_tr_b16 a[148:149], v91 offset:32\n"
    "ds_read_b64_tr_b16 a[152:153], v92 offset:32\n"
    "ds_read_b64_tr_b16 a[156:157], v93 offset:32\n"
    "ds_read_b64_tr_b16 a[160:161], v90 offset:64\n"
    "ds_read_b64_tr_b16 a[164:165], v91 offset:64\n"
    "ds_read_b64_tr_b16 a[168:169], v92 offset:64\n"
    "ds_read_b64_tr_b16 a[172:173], v93 offset:64\n"
    "ds_read_b64_tr_b16 a[176:177], v90 offset:96\n"
    "ds_read_b64_tr_b16 a[180:181], v91 offset:96\n"
    "ds_read_b64_tr_b16 a[184:185], v92 offset:96\n"
    "ds_read_b64_tr_b16 a[188:189], v93 offset:96\n"
    "ds_read_b64_tr_b16 a[192:193], v90 offset:128\n"
    "ds_read_b64_tr_b16 a[196:197], v91 offset:128\n"
    "ds_read_b64_tr_b16 a[200:201], v92 offset:128\n"
    "ds_read_b64_tr_b16 a[204:205], v93 offset:128\n"
    "ds_read_b64_tr_b16 a[208:209], v90 offset:160\n"
    "ds_read_b64_tr_b16 a[212:213], v91 offset:160\n"
    "ds_read_b64_tr_b16 a[216:217], v92 offset:160\n"
    "ds_read_b64_tr_b16 a[220:221], v93 offset:160\n"
    "ds_read_b64_tr_b16 a[224:225], v90 offset:192\n"
    "ds_read_b64_tr_b16 a[228:229], v91 offset:192\n"
    "ds_read_b64_tr_b16 a[232:233], v92 offset:192\n"
    "ds_read_b64_tr_b16 a[236:237], v93 offset:192\n"
    "ds_read_b64_tr_b16 a[240:241], v90 offset:224\n"
    "ds_read_b64_tr_b16 a[244:245], v91 offset:224\n"
    "ds_read_b64_tr_b16 a[248:249], v92 offset:224\n"
    "ds_read_b64_tr_b16 a[252:253], v93 offset:224\n"
    "v_add_u32 v89, 67584, v89\n"
    "v_add_u32 v90, 67584, v90\n"
    "v_add_u32 v91, 67584, v91\n"
    "v_add_u32 v92, 67584, v92\n"
    "v_add_u32 v93, 67584, v93\n"
    "s_waitcnt lgkmcnt(0)\n"
    "LbA:\n"
    "s_bitcmp1_b32 s61, 0\n"
    "s_cselect_b64 s[32:33], s[36:37], s[32:33]\n"
    "s_cselect_b32 s4, 1024, s61\n"
    "s_cselect_b32 s4, s4, 32768\n"
    "s_cselect_b32 s6, 0x4000, s61\n"
    "s_cselect_b32 s6, s6, 524288\n"
    "s_waitcnt vmcnt(0)\n"
    "s_barrier\n"
    "Lseg_B0_0:\n"
    "s_cbranch_execz Lseg_B0_1\n"
    "s_mul_i32 s84, s65, 2112\n"
    "s_add_u32 s93, s98, s84\n"
    "s_sub_u32 s94, s98, s84\n"
    "v_mov_b32 v226, 156672\n"
    "ds_read_b64 v[228:229], v226\n"
    "Lh10c_18:\n"
    "ds_read_b128 v[144:147], v124 offset:0\n"
    "ds_read_b128 a[64:67], v89 offset:0\n"
    "ds_read_b128 v[148:151], v124 offset:64\n"
    "ds_read_b128 a[68:71], v89 offset:64\n"
    "ds_read_b128 v[152:155], v124 offset:128\n"
    "ds_read_b128 a[72:75], v89 offset:128\n"
    "ds_read_b128 v[156:159], v124 offset:192\n"
    "ds_read_b128 a[76:79], v89 offset:192\n"
    "s_waitcnt lgkmcnt(7)\n"
    "v_mfma_f32_16x16x32_bf16 v[208:211], a[0:3], v[144:147], 0\n"
    "s_waitcnt lgkmcnt(6)\n"
    "v_mfma_f32_16x16x32_bf16 v[212:215], a[64:67], v[144:147], 0\n"
    "ds_read_b128 v[160:163], v124 offset:256\n"
    "ds_read_b128 a[80:83], v89 offset:256\n"
    "ds_read_b128 v[164:167], v124 offset:320\n"
    "s_mov_b32 s87, 0\n"
    "s_add_u32 m0, s98, 0\n"
    "s_nop 0\n"
    "buffer_load_dwordx4 v82, s[32:35], s87 offen offset:0 lds\n"
    "s_waitcnt lgkmcnt(8)\n"
    "v_mfma_f32_16x16x32_bf16 v[208:211], a[4:7], v[148:151], v[208:211]\n"
    "s_waitcnt lgkmcnt(7)\n"
    "v_mfma_f32_16x16x32_bf16 v[212:215], a[68:71], v[148:151], v[212:215]\n"
    "ds_read_b128 a[84:87], v89 offset:320\n"
    "ds_read_b128 v[168:171], v124 offset:384\n"
    "ds_read_b128 a[88:91], v89 offset:384\n"
    "s_add_u32 s87, s87, s4\n"
    "s_add_u32 m0, s98, 1056\n"
    "s_nop 0\n"
    "buffer_load_dwordx4 v82, s[32:35], s87 offen offset:0 lds\n"
    "s_waitcnt lgkmcnt(9)\n"
    "v_mfma_f32_16x16x32_bf16 v[208:211], a[8:11], v[152:155], v[208:211]\n"
    "s_waitcnt lgkmcnt(8)\n"
    "v_mfma_f32_16x16x32_bf16 v[212:215], a[72:75], v[152:155], v[212:215]\n"
    "ds_read_b128 v[172:175], v124 offset:448\n"
    "ds_read_b128 a[92:95], v89 offset:448\n"
    "ds_read_b128 v[176:179], v124 offset:512\n"
    "s_add_u32 s87, s87, s4\n"
    "s_add_u32 m0, s98, 2112\n"
    "s_nop 0\n"
    "buffer_load_dwordx4 v82, s[32:35], s87 offen offset:0 lds\n"
    "s_waitcnt lgkmcnt(10)\n"
    "v_mfma_f32_16x16x32_bf16 v[208:211], a[12:15], v[156:159], v[208:211]\n"
    "s_waitcnt lgkmcnt(9)\n"
    "v_mfma_f32_16x16x32_bf16 v[212:215], a[76:79], v[156:159], v[212:215]\n"
    "ds_read_b128 a[96:99], v89 offset:512\n"
    "ds_read_b128 v[180:183], v124 offset:576\n"
    "ds_read_b128 a[100:103], v89 offset:576\n"
    "s_add_u32 s87, s87, s4\n"
    "s_add_u32 m0, s98, 3168\n"
    "s_nop 0\n"
    "buffer_load_dwordx4 v82, s[32:35], s87 offen offset:0 lds\n"
    "s_waitcnt lgkmcnt(11)\n"
    "v_mfma_f32_16x16x32_bf16 v[208:211], a[16:19], v[160:163], v[208:211]\n"
    "s_waitcnt lgkmcnt(10)\n"
    "v_mfma_f32_16x16x32_bf16 v[212:215], a[80:83], v[160:163], v[212:215]\n"
    "ds_read_b128 v[184:187], v124 offset:640\n"
    "ds_read_b128 a[104:107], v89 offset:640\n"
    "s_mov_b32 s87, s6\n"
    "s_add_u32 m0, s98, 16896\n"
    "s_nop 0\n"
    "buffer_load_dwordx4 v82, s[32:35], s87 offen offset:0 lds\n"
    "s_waitcnt lgkmcnt(11)\n"
    "v_mfma_f32_16x16x32_bf16 v[208:211], a[20:23], v[164:167], v[208:211]\n"
    "s_waitcnt lgkmcnt(10)\n"
    "v_mfma_f32_16x16x32_bf16 v[212:215], a[84:87], v[164:167], v[212:215]\n"
    "ds_read_b128 v[188:191], v124 offset:704\n"
    "ds_read_b128 a[108:111], v89 offset:704\n"
    "s_add_u32 s87, s87, s4\n"
    "s_add_u32 m0, s98, 17952\n"
    "s_nop 0\n"
    "buffer_load_dwordx4 v82, s[32:35], s87 offen offset:0 lds\n"
    "s_waitcnt lgkmcnt(11)\n"
    "v_mfma_f32_16x16x32_bf16 v[208:211], a[24:27], v[168:171], v[208:211]\n"
    "s_waitcnt lgkmcnt(10)\n"
    "v_mfma_f32_16x16x32_bf16 v[212:215], a[88:91], v[168:171], v[212:215]\n"
    "ds_read_b128 v[192:195], v124 offset:768\n"
    "ds_read_b128 a[112:115], v89 offset:768\n"
    "s_add_u32 s87, s87, s4\n"
    "s_add_u32 m0, s98, 19008\n"
    "s_nop 0\n"
    "buffer_load_dwordx4 v82, s[32:35], s87 offen offset:0 lds\n"
    "s_waitcnt lgkmcnt(11)\n"
    "v_mfma_f32_16x16x32_bf16 v[208:211], a[28:31], v[172:175], v[208:211]\n"
    "s_waitcnt lgkmcnt(10)\n"
    "v_mfma_f32_16x16x32_bf16 v[212:215], a[92:95], v[172:175], v[212:215]\n"
    "ds_read_b128 v[196:199], v124 offset:832\n"
    "ds_read_b128 a[116:119], v89 offset:832\n"
    "s_add_u32 s87, s87, s4\n"
    "s_add_u32 m0, s98, 20064\n"
    "s_nop 0\n"
    "buffer_load_dwordx4 v82, s[32:35], s87 offen offset:0 lds\n"
    "s_waitcnt lgkmcnt(11)\n"
    "v_mfma_f32_16x16x32_bf16 v[208:211], a[32:35], v[176:179], v[208:211]\n"
    "s_waitcnt lgkmcnt(10)\n"
    "v_mfma_f32_16x16x32_bf16 v[212:215], a[96:99], v[176:179], v[212:215]\n"
    "ds_read_b128 v[200:203], v124 offset:896\n"
    "ds_read_b128 a[120:123], v89 offset:896\n"
    "s_waitcnt lgkmcnt(11)\n"
    "v_mfma_f32_16x16x32_bf16 v[208:211], a[36:39], v[180:183], v[208:211]\n"
    "s_waitcnt lgkmcnt(10)\n"
    "v_mfma_f32_16x16x32_bf16 v[212:215], a[100:103], v[180:183], v[212:215]\n"
    "ds_read_b128 v[204:207], v124 offset:960\n"
    "ds_read_b128 a[124:127], v89 offset:960\n"
    "s_waitcnt lgkmcnt(11)\n"
    "v_mfma_f32_16x16x32_bf16 v[208:211], a[40:43], v[184:187], v[208:211]\n"
    "s_waitcnt lgkmcnt(10)\n"
    "v_mfma_f32_16x16x32_bf16 v[212:215], a[104:107], v[184:187], v[212:215]\n"
    "s_waitcnt lgkmcnt(9)\n"
    "v_mfma_f32_16x16x32_bf16 v[208:211], a[44:47], v[188:191], v[208:211]\n"
    "s_waitcnt lgkmcnt(8)\n"
    "v_mfma_f32_16x16x32_bf16 v[212:215], a[108:111], v[188:191], v[212:215]\n"
    "s_waitcnt lgkmcnt(7)\n"
    "v_mfma_f32_16x16x32_bf16 v[208:211], a[48:51], v[192:195], v[208:211]\n"
    "s_waitcnt lgkmcnt(6)\n"
    "v_mfma_f32_16x16x32_bf16 v[212:215], a[112:115], v[192:195], v[212:215]\n"
    "s_waitcnt lgkmcnt(5)\n"
    "v_mfma_f32_16x16x32_bf16 v[208:211], a[52:55], v[196:199], v[208:211]\n"
    "s_waitcnt lgkmcnt(4)\n"
    "v_mfma_f32_16x16x32_bf16 v[212:215], a[116:119], v[196:199], v[212:215]\n"
    "s_waitcnt lgkmcnt(3)\n"
    "v_mfma_f32_16x16x32_bf16 v[208:211], a[56:59], v[200:203], v[208:211]\n"
    "s_waitcnt lgkmcnt(2)\n"
    "v_mfma_f32_16x16x32_bf16 v[212:215], a[120:123], v[200:203], v[212:215]\n"
    "s_waitcnt lgkmcnt(1)\n"
    "v_mfma_f32_16x16x32_bf16 v[208:211], a[60:63], v[204:207], v[208:211]\n"
    "s_waitcnt lgkmcnt(0)\n"
    "v_mfma_f32_16x16x32_bf16 v[212:215], a[124:127], v[204:207], v[212:215]\n"
    "v_mov_b32 v152, 0x3f803f80\n"
    "v_mov_b32 v153, 0x3f803f80\n"
    "v_mov_b32 v154, 0x3f803f80\n"
    "v_mov_b32 v155, 0x3f803f80\n"
    "s_nop 1\n"
    "v_max3_f32 v144, v208, v209, v210\n"
    "s_nop 0\n"
    "v_max3_f32 v145, v211, v212, v213\n"
    "v_max3_f32 v145, v145, v214, v215\n"
    "v_cmp_u_f32 vcc, v208, v212\n"
    "v_max_f32 v144, v144, v145\n"
    "v_max_f32 v145, v144, v145\n"
    "s_cbranch_vccnz Lh10s_19\n"
    "Lh10d_20:\n"
    "s_nop 0\n"
    "v_permlane32_swap_b32 v144, v145\n"
    "v_max_f32 v146, v144, v145\n"
    "v_max_f32 v147, v144, v145\n"
    "s_nop 1\n"
    "v_permlane16_swap_b32 v146, v147\n"
    "Lseg_B0_1:\n"
    "s_cbranch_execz Lseg_B0_2\n"
    "v_max_f32 v144, v146, v147\n"
    "ds_write_b32 v125, v144\n"
    "v_readfirstlane_b32 s67, v228\n"
    "v_readfirstlane_b32 s2, v229\n"
    "s_nop 0\n"
    "v_xor_b32 v117, s67, v117\n"
    "v_xor_b32 v113, s67, v113\n"
    "v_xor_b32 v112, s67, v112\n"
    "v_xor_b32 v123, s67, v123\n"
    "s_waitcnt lgkmcnt(0)\n"
    "s_barrier\n"
    "ds_read_b128 v[148:151], v126\n"
    "ds_read_b64_tr_b16 a[130:131], v90 offset:0\n"
    "ds_read_b64_tr_b16 a[146:147], v90 offset:32\n"
    "ds_read_b64_tr_b16 a[162:163], v90 offset:64\n"
    "ds_read_b64_tr_b16 a[178:179], v90 offset:96\n"
    "ds_read_b64_tr_b16 a[194:195], v90 offset:128\n"
    "ds_read_b64_tr_b16 a[210:211], v90 offset:160\n"
    "ds_read_b64_tr_b16 a[226:227], v90 offset:192\n"
    "ds_read_b64_tr_b16 a[242:243], v90 offset:224\n"
    "ds_read_b64_tr_b16 a[134:135], v91 offset:0\n"
    "ds_read_b64_tr_b16 a[150:151], v91 offset:32\n"
    "ds_read_b64_tr_b16 a[166:167], v91 offset:64\n"
    "ds_read_b64_tr_b16 a[182:183], v91 offset:96\n"
    "ds_read_b64_tr_b16 a[198:199], v91 offset:128\n"
    "ds_read_b64_tr_b16 a[214:215], v91 offset:160\n"
    "v_xor_b32 v114, s67, v114\n"
    "v_xor_b32 v115, s67, v115\n"
    "v_xor_b32 v116, s67, v116\n"
    "v_xor_b32 v118, s67, v118\n"
    "v_xor_b32 v120, s67, v120\n"
    "v_xor_b32 v122, s67, v122\n"
    "v_xor_b32 v236, s67, v236\n"
    "s_xor_b32 s68, s68, s67\n"
    "buffer_store_dwordx4 v[84:87], v80, s[12:15], 0 offen sc1\n"
    "s_waitcnt lgkmcnt(14)\n"
    "v_max3_f32 v146, v148, v149, v150\n"
    "v_max_f32 v146, v146, v151\n"
    "v_sub_f32 v208, v208, v146\n"
    "ds_read_b64_tr_b16 a[230:231], v91 offset:192\n"
    "ds_read_b64_tr_b16 a[246:247], v91 offset:224\n"
    "ds_read_b64_tr_b16 a[138:139], v92 offset:0\n"
    "v_sub_f32 v209, v209, v146\n"
    "v_exp_f32 v208, v208\n"
    "ds_read_b64_tr_b16 a[154:155], v92 offset:32\n"
    "ds_read_b64_tr_b16 a[170:171], v92 offset:64\n"
    "ds_read_b64_tr_b16 a[186:187], v92 offset:96\n"
    "v_sub_f32 v210, v210, v146\n"
    "v_exp_f32 v209, v209\n"
    "ds_read_b64_tr_b16 a[202:203], v92 offset:128\n"
    "ds_read_b64_tr_b16 a[218:219], v92 offset:160\n"
    "ds_read_b64_tr_b16 a[234:235], v92 offset:192\n"
    "v_sub_f32 v211, v211, v146\n"
    "v_exp_f32 v210, v210\n"
    "ds_read_b64_tr_b16 a[250:251], v92 offset:224\n"
    "ds_read_b64_tr_b16 a[142:143], v93 offset:0\n"
    "ds_read_b64_tr_b16 a[158:159], v93 offset:32\n"
    "v_sub_f32 v212, v212, v146\n"
    "v_exp_f32 v211, v211\n"
    "ds_read_b64_tr_b16 a[174:175], v93 offset:64\n"
    "ds_read_b64_tr_b16 a[190:191], v93 offset:96\n"
    "ds_read_b64_tr_b16 a[206:207], v93 offset:128\n"
    "v_sub_f32 v213, v213, v146\n"
    "v_exp_f32 v212, v212\n"
    "ds_read_b64_tr_b16 a[222:223], v93 offset:160\n"
    "ds_read_b64_tr_b16 a[238:239], v93 offset:192\n"
    "ds_read_b64_tr_b16 a[254:255], v93 offset:224\n"
    "v_sub_f32 v214, v214, v146\n"
    "v_exp_f32 v213, v213\n"
    "v_sub_f32 v215, v215, v146\n"
    "v_exp_f32 v214, v214\n"
    "v_exp_f32 v215, v215\n"
    "v_cvt_pk_bf16_f32 v216, v208, v209\n"
    "v_cvt_pk_bf16_f32 v217, v210, v211\n"
    "v_cvt_pk_bf16_f32 v218, v212, v213\n"
    "v_cvt_pk_bf16_f32 v219, v214, v215\n"
    "ds_write_b128 v127, v[216:219]\n"
    "s_waitcnt lgkmcnt(15)\n"
    "v_mfma_f32_16x16x32_bf16 v[168:171], v[216:219], a[128:131], 0\n"
    "v_mfma_f32_16x16x32_bf16 v[172:175], v[216:219], a[144:147], 0\n"
    "v_mfma_f32_16x16x32_bf16 v[176:179], v[216:219], a[160:163], 0\n"
    "s_waitcnt lgkmcnt(0)\n"
    "s_barrier\n"
    "ds_read_b128 v[156:159], v128\n"
    "ds_read_b128 v[160:163], v129\n"
    "ds_read_b128 v[164:167], v130\n"
    "v_mfma_f32_16x16x32_bf16 v[180:183], v[216:219], a[176:179], 0\n"
    "v_mfma_f32_16x16x32_bf16 v[184:187], v[216:219], a[192:195], 0\n"
    "v_mfma_f32_16x16x32_bf16 v[188:191], v[216:219], a[208:211], 0\n"
    "v_mfma_f32_16x16x32_bf16 v[192:195], v[216:219], a[224:227], 0\n"
    "v_mfma_f32_16x16x32_bf16 v[196:199], v[216:219], a[240:243], 0\n"
    "s_waitcnt lgkmcnt(2)\n"
    "v_mfma_f32_16x16x32_bf16 v[168:171], v[156:159], a[132:135], v[168:171]\n"
    "v_mfma_f32_16x16x32_bf16 v[172:175], v[156:159], a[148:151], v[172:175]\n"
    "v_mfma_f32_16x16x32_bf16 v[176:179], v[156:159], a[164:167], v[176:179]\n"
    "v_mfma_f32_16x16x32_bf16 v[180:183], v[156:159], a[180:183], v[180:183]\n"
    "s_waitcnt lgkmcnt(1)\n"
    "v_mfma_f32_16x16x32_bf16 v[168:171], v[160:163], a[136:139], v[168:171]\n"
    "v_mfma_f32_16x16x32_bf16 v[172:175], v[160:163], a[152:155], v[172:175]\n"
    "v_mfma_f32_16x16x32_bf16 v[176:179], v[160:163], a[168:171], v[176:179]\n"
    "v_mfma_f32_16x16x32_bf16 v[180:183], v[160:163], a[184:187], v[180:183]\n"
    "s_waitcnt lgkmcnt(0)\n"
    "v_mfma_f32_16x16x32_bf16 v[168:171], v[164:167], a[140:143], v[168:171]\n"
    "v_mfma_f32_16x16x32_bf16 v[172:175], v[164:167], a[156:159], v[172:175]\n"
    "v_mfma_f32_16x16x32_bf16 v[176:179], v[164:167], a[172:175], v[176:179]\n"
    "v_mfma_f32_16x16x32_bf16 v[180:183], v[164:167], a[188:191], v[180:183]\n"
    "v_mfma_f32_16x16x32_bf16 v[200:203], v[152:155], v[216:219], 0\n"
    "v_mfma_f32_16x16x32_bf16 v[200:203], v[152:155], v[156:159], v[200:203]\n"
    "v_mfma_f32_16x16x32_bf16 v[200:203], v[152:155], v[160:163], v[200:203]\n"
    "s_nop 2\n"
    "v_cvt_pk_bf16_f32 v204, v168, v172\n"
    "s_nop 0\n"
    "v_cvt_pk_bf16_f32 v205, v176, v180\n"
    "buffer_store_dwordx2 v[204:205], v117, s[12:15], s70 offen sc1\n"
    "v_mfma_f32_16x16x32_bf16 v[200:203], v[152:155], v[164:167], v[200:203]\n"
    "v_mfma_f32_16x16x32_bf16 v[184:187], v[156:159], a[196:199], v[184:187]\n"
    "v_cvt_pk_bf16_f32 v206, v169, v173\n"
    "v_cvt_pk_bf16_f32 v207, v177, v181\n"
    "buffer_store_dwordx2 v[206:207], v117, s[12:15], s71 offen sc1\n"
    "v_mfma_f32_16x16x32_bf16 v[188:191], v[156:159], a[212:215], v[188:191]\n"
    "v_mfma_f32_16x16x32_bf16 v[192:195], v[156:159], a[228:231], v[192:195]\n"
    "v_cvt_pk_bf16_f32 v208, v170, v174\n"
    "v_cvt_pk_bf16_f32 v209, v178, v182\n"
    "buffer_store_dwordx2 v[208:209], v117, s[12:15], s72 offen sc1\n"
    "v_mfma_f32_16x16x32_bf16 v[196:199], v[156:159], a[244:247], v[196:199]\n"
    "v_mfma_f32_16x16x32_bf16 v[184:187], v[160:163], a[200:203], v[184:187]\n"
    "v_cvt_pk_bf16_f32 v210, v171, v175\n"
    "v_cvt_pk_bf16_f32 v211, v179, v183\n"
    "buffer_store_dwordx2 v[210:211], v117, s[12:15], s73 offen sc1\n"
    "v_mfma_f32_16x16x32_bf16 v[188:191], v[160:163], a[216:219], v[188:191]\n"
    "v_mfma_f32_16x16x32_bf16 v[192:195], v[160:163], a[232:235], v[192:195]\n"
    "v_mov_b32 v147, v200\n"
    "v_mfma_f32_16x16x32_bf16 v[196:199], v[160:163], a[248:251], v[196:199]\n"
    "v_mfma_f32_16x16x32_bf16 v[184:187], v[164:167], a[204:207], v[184:187]\n"
    "v_mfma_f32_16x16x32_bf16 v[188:191], v[164:167], a[220:223], v[188:191]\n"
    "v_mfma_f32_16x16x32_bf16 v[192:195], v[164:167], a[236:239], v[192:195]\n"
    "v_mfma_f32_16x16x32_bf16 v[196:199], v[164:167], a[252:255], v[196:199]\n"
    "s_nop 5\n"
    "v_cvt_pk_bf16_f32 v220, v184, v188\n"
    "s_nop 0\n"
    "v_cvt_pk_bf16_f32 v221, v192, v196\n"
    "buffer_store_dwordx2 v[220:221], v117, s[12:15], s74 offen sc1\n"
    "v_cvt_pk_bf16_f32 v222, v185, v189\n"
    "v_cvt_pk_bf16_f32 v223, v193, v197\n"
    "buffer_store_dwordx2 v[222:223], v117, s[12:15], s75 offen sc1\n"
    "v_cvt_pk_bf16_f32 v224, v186, v190\n"
    "v_cvt_pk_bf16_f32 v225, v194, v198\n"
    "buffer_store_dwordx2 v[224:225], v117, s[12:15], s76 offen sc1\n"
    "v_cvt_pk_bf16_f32 v226, v187, v191\n"
    "v_cvt_pk_bf16_f32 v227, v195, v199\n"
    "buffer_store_dwordx2 v[226:227], v117, s[12:15], s77 offen sc1\n"
    "Lseg_B0_2:\n"
    "s_cbranch_execz Ltret\n"
    "s_barrier\n"
    "s_bitcmp1_b32 s61, 0\n"
    "s_cbranch_scc1 Lcf_21\n"
    "s_waitcnt vmcnt(0)\n"
    "s_branch Lcj_22\n"
    "Lcf_21:\n"
    "s_mov_b32 m0, 156928\n"
    "s_nop 0\n"
    "buffer_load_dword v247, s[48:51], 0 offen lds\n"
    "s_waitcnt vmcnt(1)\n"
    "Lcj_22:\n"
    "s_barrier\n"
    "s_memrealtime s[78:79]\n"
    "buffer_store_dwordx2 v[146:147], v119, s[12:15], s83 offen sc1\n"
    "s_waitcnt lgkmcnt(0)\n"
    "s_min_u32 s11, s78, 0xfffffffe\n"
    "v_mov_b32 v232, s11\n"
    "buffer_store_dword v232, v88, s[12:15], 0 offen sc1\n"
    "s_bitcmp1_b32 s61, 0\n"
    "s_cbranch_scc1 Lfc_23\n"
    "Lseg_C0_0:\n"
    "s_cbranch_execz Lseg_C0_1\n"
    "s_lshl_b32 s5, s59, 13\n"
    "s_bfe_u32 s85, s61, 0x20001\n"
    "s_xor_b32 s85, s85, 0\n"
    "s_lshl_b32 s88, s85, 10\n"
    "s_add_u32 s92, s88, 32768\n"
    "s_bfe_u32 s85, s61, 0x20001\n"
    "s_xor_b32 s85, s85, 1\n"
    "s_lshl_b32 s89, s85, 10\n"
    "s_add_u32 s93, s89, 32768\n"
    "s_bfe_u32 s85, s61, 0x20001\n"
    "s_xor_b32 s85, s85, 2\n"
    "s_lshl_b32 s90, s85, 10\n"
    "s_add_u32 s94, s90, 32768\n"
    "s_bfe_u32 s85, s61, 0x20001\n"
    "s_xor_b32 s85, s85, 3\n"
    "s_lshl_b32 s91, s85, 10\n"
    "s_add_u32 s95, s91, 32768\n"
    "s_lshl_b32 s84, s97, 7\n"
    "s_lshl_b32 s86, s57, 2\n"
    "s_add_u32 s84, s84, s86\n"
    "s_add_u32 s87, s84, 0x148000\n"
    "v_lshl_add_u32 v243, v0, 13, s87\n"
    "v_mov_b32 v228, 0x80000000\n"
    "v_cmp_gt_u32 vcc, 2, v0\n"
    "s_nop 1\n"
    "v_cndmask_b32 v243, v228, v243, vcc\n"
    "v_lshrrev_b32 v229, 2, v1\n"
    "s_lshl_b32 s84, s57, 19\n"
    "s_lshl_b32 s86, s59, 17\n"
    "s_add_u32 s84, s84, s86\n"
    "s_lshl_b32 s86, s97, 6\n"
    "s_add_u32 s84, s84, s86\n"
    "v_lshl_add_u32 v237, v229, 10, s84\n"
    "v_lshl_add_u32 v237, v10, 4, v237\n"
    "s_mov_b32 s87, 0\n"
    "s_add_u32 m0, s5, 33792\n"
    "s_nop 0\n"
    "buffer_load_dwordx4 v237, s[36:39], s87 offen offset:0 lds\n"
    "s_mov_b32 s87, 16384\n"
    "s_add_u32 m0, s5, 34816\n"
    "s_nop 0\n"
    "buffer_load_dwordx4 v237, s[36:39], s87 offen offset:0 lds\n"
    "s_mov_b32 s87, 32768\n"
    "s_add_u32 m0, s5, 35840\n"
    "s_nop 0\n"
    "buffer_load_dwordx4 v237, s[36:39], s87 offen offset:0 lds\n"
    "s_mov_b32 s87, 49152\n"
    "s_add_u32 m0, s5, 36864\n"
    "s_nop 0\n"
    "buffer_load_dwordx4 v237, s[36:39], s87 offen offset:0 lds\n"
    "s_mov_b32 s87, 65536\n"
    "s_add_u32 m0, s5, 37888\n"
    "s_nop 0\n"
    "buffer_load_dwordx4 v237, s[36:39], s87 offen offset:0 lds\n"
    "s_mov_b32 s87, 81920\n"
    "s_add_u32 m0, s5, 38912\n"
    "s_nop 0\n"
    "buffer_load_dwordx4 v237, s[36:39], s87 offen offset:0 lds\n"
    "s_mov_b32 s87, 98304\n"
    "s_add_u32 m0, s5, 39936\n"
    "s_nop 0\n"
    "buffer_load_dwordx4 v237, s[36:39], s87 offen offset:0 lds\n"
    "s_mov_b32 s87, 114688\n"
    "s_add_u32 m0, s5, 40960\n"
    "s_nop 0\n"
    "buffer_load_dwordx4 v237, s[36:39], s87 offen offset:0 lds\n"
    "v_xor_b32 v225, s59, v3\n"
    "v_mul_u32_u24 v230, 4224, v9\n"
    "v_add_u32 v224, 0, v230\n"
    "v_lshl_add_u32 v224, v3, 8, v224\n"
    "v_lshl_add_u32 v224, v10, 4, v224\n"
    "v_lshl_add_u32 v224, v225, 6, v224\n"
    "ds_read_b64_tr_b16 v[48:49], v224 offset:0\n"
    "ds_read_b64_tr_b16 v[50:51], v224 offset:1056\n"
    "ds_read_b64_tr_b16 v[52:53], v224 offset:2112\n"
    "ds_read_b64_tr_b16 v[54:55], v224 offset:3168\n"
    "ds_read_b64_tr_b16 v[56:57], v224 offset:16896\n"
    "ds_read_b64_tr_b16 v[58:59], v224 offset:17952\n"
    "ds_read_b64_tr_b16 v[60:61], v224 offset:19008\n"
    "ds_read_b64_tr_b16 v[62:63], v224 offset:20064\n"
    "ds_read_b64_tr_b16 v[64:65], v224 offset:8\n"
    "ds_read_b64_tr_b16 v[66:67], v224 offset:1064\n"
    "ds_read_b64_tr_b16 v[68:69], v224 offset:2120\n"
    "ds_read_b64_tr_b16 v[70:71], v224 offset:3176\n"
    "ds_read_b64_tr_b16 v[72:73], v224 offset:16904\n"
    "ds_read_b64_tr_b16 v[74:75], v224 offset:17960\n"
    "ds_read_b64_tr_b16 v[76:77], v224 offset:19016\n"
    "ds_read_b64_tr_b16 v[78:79], v224 offset:20072\n"
    "s_sleep 2\n"
    "buffer_load_dwordx2 v[144:145], v120, s[12:15], s83 offen sc1\n"
    "Lh20c_26:\n"
    "s_waitcnt vmcnt(0)\n"
    "v_cmp_u_f32 vcc, v144, v145\n"
    "s_cbranch_vccnz Lh20s_27\n"
    "Lh20d_28:\n"
    "s_add_u32 m0, s5, 67584\n"
    "s_nop 0\n"
    "buffer_load_dwordx4 v118, s[12:15], s88 offen offset:0 sc1 lds\n"
    "s_add_u32 m0, s5, 68608\n"
    "s_nop 0\n"
    "buffer_load_dwordx4 v118, s[12:15], s89 offen offset:0 sc1 lds\n"
    "s_add_u32 m0, s5, 69632\n"
    "s_nop 0\n"
    "buffer_load_dwordx4 v118, s[12:15], s90 offen offset:0 sc1 lds\n"
    "s_add_u32 m0, s5, 70656\n"
    "s_nop 0\n"
    "buffer_load_dwordx4 v118, s[12:15], s91 offen offset:0 sc1 lds\n"
    "s_add_u32 m0, s5, 71680\n"
    "s_nop 0\n"
    "buffer_load_dwordx4 v118, s[12:15], s92 offen offset:0 sc1 lds\n"
    "s_add_u32 m0, s5, 72704\n"
    "s_nop 0\n"
    "buffer_load_dwordx4 v118, s[12:15], s93 offen offset:0 sc1 lds\n"
    "s_add_u32 m0, s5, 73728\n"
    "s_nop 0\n"
    "buffer_load_dwordx4 v118, s[12:15], s94 offen offset:0 sc1 lds\n"
    "s_add_u32 m0, s5, 74752\n"
    "s_nop 0\n"
    "buffer_load_dwordx4 v118, s[12:15], s95 offen offset:0 sc1 lds\n"
    "s_barrier\n"
    "buffer_load_dwordx4 v[232:235], v236, s[12:15], 0 offen sc1\n"
    "v_max_f32_dpp v146, v144, v144 row_ror:1 row_mask:0xf bank_mask:0xf\n"
    "s_nop 1\n"
    "v_max_f32_dpp v146, v146, v146 row_ror:2 row_mask:0xf bank_mask:0xf\n"
    "s_nop 1\n"
    "v_max_f32_dpp v146, v146, v146 row_ror:4 row_mask:0xf bank_mask:0xf\n"
    "s_nop 1\n"
    "v_max_f32_dpp v146, v146, v146 row_ror:8 row_mask:0xf bank_mask:0xf\n"
    "v_mov_b32 v147, v146\n"
    "s_nop 1\n"
    "v_permlane16_swap_b32 v146, v147\n"
    "v_max_f32 v146, v146, v147\n"
    "v_sub_f32 v148, v144, v146\n"
    "v_exp_f32 v148, v148\n"
    "s_nop 0\n"
    "v_mul_f32 v150, v148, v145\n"
    "s_nop 1\n"
    "v_add_f32_dpp v149, v150, v150 row_ror:1 row_mask:0xf bank_mask:0xf\n"
    "Lseg_C0_1:\n"
    "s_cbranch_execz Lseg_C0_2\n"
    "s_nop 1\n"
    "v_add_f32_dpp v149, v149, v149 row_ror:2 row_mask:0xf bank_mask:0xf\n"
    "s_nop 1\n"
    "v_add_f32_dpp v149, v149, v149 row_ror:4 row_mask:0xf bank_mask:0xf\n"
    "s_nop 1\n"
    "v_add_f32_dpp v149, v149, v149 row_ror:8 row_mask:0xf bank_mask:0xf\n"
    "v_mov_b32 v147, v149\n"
    "s_nop 1\n"
    "v_permlane16_swap_b32 v149, v147\n"
    "v_add_f32 v149, v149, v147\n"
    "v_rcp_f32 v149, v149\n"
    "s_nop 0\n"
    "v_mul_f32 v148, v148, v149\n"
    "v_cvt_pk_bf16_f32 v148, v148, v148\n"
    "ds_write_b16 v135, v148\n"
    "ds_read_b128 v[152:155], v136\n"
    "s_waitcnt vmcnt(7)\n"
    "ds_read_b64_tr_b16 v[156:157], v131 offset:0\n"
    "ds_read_b64_tr_b16 v[160:161], v132 offset:0\n"
    "ds_read_b64_tr_b16 v[164:165], v133 offset:0\n"
    "ds_read_b64_tr_b16 v[168:169], v134 offset:0\n"
    "s_waitcnt vmcnt(5)\n"
    "ds_read_b64_tr_b16 v[158:159], v131 offset:2048\n"
    "ds_read_b64_tr_b16 v[162:163], v132 offset:2048\n"
    "ds_read_b64_tr_b16 v[166:167], v133 offset:2048\n"
    "ds_read_b64_tr_b16 v[170:171], v134 offset:2048\n"
    "s_waitcnt lgkmcnt(3)\n"
    "v_mfma_f32_16x16x32_bf16 v[188:191], v[156:159], v[152:155], 0\n"
    "s_waitcnt lgkmcnt(2)\n"
    "v_mfma_f32_16x16x32_bf16 v[192:195], v[160:163], v[152:155], 0\n"
    "s_waitcnt lgkmcnt(1)\n"
    "v_mfma_f32_16x16x32_bf16 v[196:199], v[164:167], v[152:155], 0\n"
    "s_waitcnt lgkmcnt(0)\n"
    "v_mfma_f32_16x16x32_bf16 v[200:203], v[168:171], v[152:155], 0\n"
    "s_nop 1\n"
    "v_cvt_pk_bf16_f32 v204, v188, v189\n"
    "v_cvt_pk_bf16_f32 v205, v190, v191\n"
    "v_cvt_pk_bf16_f32 v206, v192, v193\n"
    "v_cvt_pk_bf16_f32 v207, v194, v195\n"
    "s_mov_b32 exec_lo, 0x00010001\n"
    "s_mov_b32 exec_hi, 0x00010001\n"
    "ds_write_b128 v137, v[204:207] offset:0\n"
    "v_cvt_pk_bf16_f32 v208, v196, v197\n"
    "v_cvt_pk_bf16_f32 v209, v198, v199\n"
    "v_cvt_pk_bf16_f32 v210, v200, v201\n"
    "v_cvt_pk_bf16_f32 v211, v202, v203\n"
    "ds_write_b128 v137, v[208:211] offset:256\n"
    "s_mov_b64 exec, -1\n"
    "s_waitcnt vmcnt(3)\n"
    "ds_read_b64_tr_b16 v[172:173], v131 offset:4096\n"
    "ds_read_b64_tr_b16 v[176:177], v132 offset:4096\n"
    "ds_read_b64_tr_b16 v[180:181], v133 offset:4096\n"
    "ds_read_b64_tr_b16 v[184:185], v134 offset:4096\n"
    "s_waitcnt vmcnt(1)\n"
    "ds_read_b64_tr_b16 v[174:175], v131 offset:6144\n"
    "ds_read_b64_tr_b16 v[178:179], v132 offset:6144\n"
    "ds_read_b64_tr_b16 v[182:183], v133 offset:6144\n"
    "ds_read_b64_tr_b16 v[186:187], v134 offset:6144\n"
    "s_waitcnt lgkmcnt(3)\n"
    "v_mfma_f32_16x16x32_bf16 v[188:191], v[172:175], v[152:155], 0\n"
    "s_waitcnt lgkmcnt(2)\n"
    "v_mfma_f32_16x16x32_bf16 v[192:195], v[176:179], v[152:155], 0\n"
    "s_waitcnt lgkmcnt(1)\n"
    "v_mfma_f32_16x16x32_bf16 v[196:199], v[180:183], v[152:155], 0\n"
    "s_waitcnt lgkmcnt(0)\n"
    "v_mfma_f32_16x16x32_bf16 v[200:203], v[184:187], v[152:155], 0\n"
    "s_nop 1\n"
    "v_cvt_pk_bf16_f32 v204, v188, v189\n"
    "v_cvt_pk_bf16_f32 v205, v190, v191\n"
    "v_cvt_pk_bf16_f32 v206, v192, v193\n"
    "v_cvt_pk_bf16_f32 v207, v194, v195\n"
    "s_mov_b32 exec_lo, 0x00010001\n"
    "s_mov_b32 exec_hi, 0x00010001\n"
    "ds_write_b128 v137, v[204:207] offset:512\n"
    "v_cvt_pk_bf16_f32 v208, v196, v197\n"
    "v_cvt_pk_bf16_f32 v209, v198, v199\n"
    "v_cvt_pk_bf16_f32 v210, v200, v201\n"
    "v_cvt_pk_bf16_f32 v211, v202, v203\n"
    "ds_write_b128 v137, v[208:211] offset:768\n"
    "s_mov_b64 exec, -1\n"
    "s_waitcnt lgkmcnt(0)\n"
    "s_barrier\n"
    "ds_read_b128 v[160:163], v138 offset:0\n"
    "ds_read_b128 v[164:167], v138 offset:256\n"
    "ds_read_b128 v[168:171], v138 offset:512\n"
    "ds_read_b128 v[172:175], v138 offset:768\n"
    "Lbcc_29:\n"
    "s_waitcnt vmcnt(0)\n"
    "v_max3_u32 v230, v232, v233, v234\n"
    "v_max_u32 v230, v230, v235\n"
    "v_cmp_eq_u32 vcc, -1, v230\n"
    "s_cbranch_vccnz Lbcs_30\n"
    "Lbcd_31:\n"
    "s_waitcnt lgkmcnt(3)\n"
    "v_mfma_f32_16x16x32_bf16 v[220:223], v[48:51], v[160:163], 0\n"
    "v_mfma_f32_16x16x32_bf16 v[224:227], v[64:67], v[160:163], 0\n"
    "s_waitcnt lgkmcnt(2)\n"
    "v_mfma_f32_16x16x32_bf16 v[220:223], v[52:55], v[164:167], v[220:223]\n"
    "v_mfma_f32_16x16x32_bf16 v[224:227], v[68:71], v[164:167], v[224:227]\n"
    "s_waitcnt lgkmcnt(1)\n"
    "v_mfma_f32_16x16x32_bf16 v[220:223], v[56:59], v[168:171], v[220:223]\n"
    "v_mfma_f32_16x16x32_bf16 v[224:227], v[72:75], v[168:171], v[224:227]\n"
    "s_waitcnt lgkmcnt(0)\n"
    "v_mfma_f32_16x16x32_bf16 v[220:223], v[60:63], v[172:175], v[220:223]\n"
    "v_mfma_f32_16x16x32_bf16 v[224:227], v[76:79], v[172:175], v[224:227]\n"
    "s_nop 6\n"
    "v_cvt_pk_bf16_f32 v156, v220, v221\n"
    "v_cvt_pk_bf16_f32 v157, v222, v223\n"
    "v_cvt_pk_bf16_f32 v158, v224, v225\n"
    "v_cvt_pk_bf16_f32 v159, v226, v227\n"
    "buffer_store_dwordx4 v[156:159], v123, s[12:15], s83 offen sc1\n"
    "s_memrealtime s[80:81]\n"
    "s_waitcnt lgkmcnt(0)\n"
    "s_min_u32 s80, s80, 0xfffffffe\n"
    "v_mov_b32 v228, s80\n"
    "buffer_store_dword v228, v243, s[12:15], 0 offen sc1\n"
    "v_mov_b32 v184, 0x3f803f80\n"
    "v_mov_b32 v185, 0x3f803f80\n"
    "v_mov_b32 v186, 0x3f803f80\n"
    "v_mov_b32 v187, 0x3f803f80\n"
    "s_nop 1\n"
    "v_mfma_f32_16x16x32_bf16 v[188:191], v[16:19], v[184:187], 0\n"
    "v_mfma_f32_16x16x32_bf16 v[188:191], v[20:23], v[184:187], v[188:191]\n"
    "v_mfma_f32_16x16x32_bf16 v[188:191], v[24:27], v[184:187], v[188:191]\n"
    "v_mfma_f32_16x16x32_bf16 v[188:191], v[28:31], v[184:187], v[188:191]\n"
    "v_subrev_u32 v232, s11, v232\n"
    "v_subrev_u32 v233, s11, v233\n"
    "v_subrev_u32 v234, s11, v234\n"
    "v_subrev_u32 v235, s11, v235\n"
    "v_max3_i32 v226, v232, v233, v234\n"
    "v_max_i32 v226, v226, v235\n"
    "s_nop 1\n"
    "v_max_i32_dpp v226, v226, v226 row_ror:1 row_mask:0xf bank_mask:0xf\n"
    "s_nop 1\n"
    "v_max_i32_dpp v226, v226, v226 row_ror:2 row_mask:0xf bank_mask:0xf\n"
    "s_nop 1\n"
    "v_max_i32_dpp v226, v226, v226 row_ror:4 row_mask:0xf bank_mask:0xf\n"
    "s_nop 1\n"
    "v_max_i32_dpp v226, v226, v226 row_ror:8 row_mask:0xf bank_mask:0xf\n"
    "v_mov_b32 v227, v226\n"
    "s_nop 1\n"
    "v_permlane16_swap_b32 v226, v227\n"
    "v_max_i32 v226, v226, v227\n"
    "v_mov_b32 v227, v226\n"
    "s_nop 1\n"
    "v_permlane32_swap_b32 v226, v227\n"
    "v_max_i32 v226, v226, v227\n"
    "s_nop 0\n"
    "v_readfirstlane_b32 s84, v226\n"
    "s_add_u32 s11, s11, s84\n"
    "v_cvt_pk_bf16_f32 v196, v188, v189\n"
    "v_cvt_pk_bf16_f32 v197, v190, v191\n"
    "s_mov_b32 s84, 0x50000\n"
    "s_nop 0\n"
    "buffer_store_dwordx2 v[196:197], v112, s[12:15], s84 offen sc1\n"
    "v_add_u32 v4, s84, v113\n"
    "s_add_u32 s87, s5, 0x8400\n"
    "v_lshl_add_u32 v224, v3, 9, s87\n"
    "v_lshl_add_u32 v224, v9, 6, v224\n"
    "v_lshl_add_u32 v224, v10, 3, v224\n"
    "s_bfe_u32 s85, s57, 0x20001\n"
    "Lseg_C0_2:\n"
    "s_cbranch_execz Ltret\n"
    "s_lshl_b32 s84, s85, 14\n"
    "s_bfe_u32 s85, s57, 0x10000\n"
    "s_lshl_b32 s86, s85, 12\n"
    "s_add_u32 s84, s84, s86\n"
    "s_lshl_b32 s86, s97, 8\n"
    "s_add_u32 s84, s84, s86\n"
    "s_lshl_b32 s86, s59, 7\n"
    "s_add_u32 s84, s84, s86\n"
    "s_add_u32 s84, s84, s83\n"
    "s_add_u32 s95, s84, 0x20000\n"
    "Lwt_32:\n"
    "s_memrealtime s[78:79]\n"
    "s_waitcnt lgkmcnt(0)\n"
    "s_sub_u32 s78, s78, s11\n"
    "s_cmp_lt_u32 s78, 176\n"
    "s_cbranch_scc1 Lwt_32\n"
    "buffer_load_dwordx4 v[160:163], v114, s[12:15], s83 offen offset:0 sc1\n"
    "s_nop 11\n"
    "buffer_load_dwordx4 v[164:167], v114, s[12:15], s83 offen offset:1024 sc1\n"
    "s_nop 11\n"
    "buffer_load_dwordx4 v[168:171], v114, s[12:15], s83 offen offset:2048 sc1\n"
    "s_nop 11\n"
    "buffer_load_dwordx4 v[172:175], v114, s[12:15], s83 offen offset:3072 sc1\n"
    "ds_read_b64_tr_b16 v[80:81], v224 offset:0\n"
    "ds_read_b64_tr_b16 v[82:83], v224 offset:256\n"
    "ds_read_b64_tr_b16 v[84:85], v224 offset:32\n"
    "ds_read_b64_tr_b16 v[86:87], v224 offset:288\n"
    "ds_read_b64_tr_b16 v[88:89], v224 offset:2048\n"
    "ds_read_b64_tr_b16 v[90:91], v224 offset:2304\n"
    "ds_read_b64_tr_b16 v[92:93], v224 offset:2080\n"
    "ds_read_b64_tr_b16 v[94:95], v224 offset:2336\n"
    "ds_read_b64_tr_b16 v[96:97], v224 offset:4096\n"
    "ds_read_b64_tr_b16 v[98:99], v224 offset:4352\n"
    "ds_read_b64_tr_b16 v[100:101], v224 offset:4128\n"
    "ds_read_b64_tr_b16 v[102:103], v224 offset:4384\n"
    "ds_read_b64_tr_b16 v[104:105], v224 offset:6144\n"
    "ds_read_b64_tr_b16 v[106:107], v224 offset:6400\n"
    "ds_read_b64_tr_b16 v[108:109], v224 offset:6176\n"
    "ds_read_b64_tr_b16 v[110:111], v224 offset:6432\n"
    "Lh30c_33:\n"
    "s_waitcnt vmcnt(3)\n"
    "s_waitcnt lgkmcnt(14)\n"
    "v_mfma_f32_16x16x32_bf16 v[176:179], v[160:163], v[80:83], 0\n"
    "s_waitcnt lgkmcnt(12)\n"
    "v_mfma_f32_16x16x32_bf16 v[180:183], v[160:163], v[84:87], 0\n"
    "s_waitcnt vmcnt(2)\n"
    "s_waitcnt lgkmcnt(10)\n"
    "v_mfma_f32_16x16x32_bf16 v[176:179], v[164:167], v[88:91], v[176:179]\n"
    "s_waitcnt lgkmcnt(8)\n"
    "v_mfma_f32_16x16x32_bf16 v[180:183], v[164:167], v[92:95], v[180:183]\n"
    "s_waitcnt vmcnt(1)\n"
    "s_waitcnt lgkmcnt(6)\n"
    "v_mfma_f32_16x16x32_bf16 v[176:179], v[168:171], v[96:99], v[176:179]\n"
    "s_waitcnt lgkmcnt(4)\n"
    "v_mfma_f32_16x16x32_bf16 v[180:183], v[168:171], v[100:103], v[180:183]\n"
    "s_waitcnt vmcnt(0)\n"
    "s_waitcnt lgkmcnt(2)\n"
    "v_mfma_f32_16x16x32_bf16 v[176:179], v[172:175], v[104:107], v[176:179]\n"
    "s_waitcnt lgkmcnt(0)\n"
    "v_mfma_f32_16x16x32_bf16 v[180:183], v[172:175], v[108:111], v[180:183]\n"
    "s_nop 5\n"
    "v_permlane32_swap_b32 v176, v178\n"
    "v_permlane32_swap_b32 v177, v179\n"
    "v_permlane32_swap_b32 v180, v182\n"
    "v_permlane32_swap_b32 v181, v183\n"
    "v_add_f32 v176, v176, v178\n"
    "v_add_f32 v177, v177, v179\n"
    "v_add_f32 v180, v180, v182\n"
    "v_add_f32 v181, v181, v183\n"
    "v_permlane16_swap_b32 v176, v177\n"
    "s_nop 0\n"
    "v_permlane16_swap_b32 v180, v181\n"
    "v_add_f32 v176, v176, v177\n"
    "v_add_f32 v180, v180, v181\n"
    "ds_write2st64_b32 v139, v176, v180 offset0:0 offset1:1\n"
    "v_cmp_u_f32 vcc, v176, v176\n"
    "s_cbranch_vccnz Lh30s_34\n"
    "Lh30d_35:\n"
    "s_cmp_lt_u32 s59, 2\n"
    "s_waitcnt lgkmcnt(0)\n"
    "s_barrier\n"
    "s_cbranch_scc0 Lsk_36\n"
    "ds_read2st64_b32 v[160:161], v141 offset0:0 offset1:4\n"
    "ds_read2st64_b32 v[162:163], v141 offset0:8 offset1:12\n"
    "s_waitcnt lgkmcnt(0)\n"
    "v_pk_add_f32 v[160:161], v[160:161], v[162:163]\n"
    "v_add_f32 v160, v160, v161\n"
    "s_nop 1\n"
    "v_mov_b32_dpp v161, v160 quad_perm:[0,0,2,2] row_mask:0xf bank_mask:0xf\n"
    "v_mov_b32_dpp v162, v160 quad_perm:[1,1,3,3] row_mask:0xf bank_mask:0xf\n"
    "v_cvt_pk_bf16_f32 v161, v161, v162\n"
    "buffer_store_dword v161, v121, s[12:15], s95 offen sc1\n"
    "Lsk_36:\n"
    "s_nop 0\n"
    "v_bfe_u32 v209, v1, 1, 4\n"
    "v_and_b32 v210, 1, v1\n"
    "s_add_u32 s87, s68, 0x148000\n"
    "v_lshl_add_u32 v208, v209, 7, s87\n"
    "v_lshl_add_u32 v208, v210, 4, v208\n"
    "Lwt_37:\n"
    "s_memrealtime s[78:79]\n"
    "s_waitcnt lgkmcnt(0)\n"
    "s_sub_u32 s78, s78, s11\n"
    "s_cmp_lt_u32 s78, 192\n"
    "s_cbranch_scc1 Lwt_37\n"
    "buffer_load_dwordx4 v[204:207], v208, s[12:15], 0 offen sc1\n"
    "Lac0c_38:\n"
    "s_waitcnt vmcnt(0)\n"
    "v_max3_u32 v209, v204, v205, v206\n"
    "v_max_u32 v209, v209, v207\n"
    "v_cmp_eq_u32 vcc, -1, v209\n"
    "s_cbranch_vccnz Lac0s_39\n"
    "Lac0d_40:\n"
    "v_subrev_u32 v204, s11, v204\n"
    "v_subrev_u32 v205, s11, v205\n"
    "v_subrev_u32 v206, s11, v206\n"
    "v_subrev_u32 v207, s11, v207\n"
    "v_max3_i32 v209, v204, v205, v206\n"
    "v_max_i32 v209, v209, v207\n"
    "s_nop 1\n"
    "v_max_i32_dpp v209, v209, v209 row_ror:1 row_mask:0xf bank_mask:0xf\n"
    "s_nop 1\n"
    "v_max_i32_dpp v209, v209, v209 row_ror:2 row_mask:0xf bank_mask:0xf\n"
    "s_nop 1\n"
    "v_max_i32_dpp v209, v209, v209 row_ror:4 row_mask:0xf bank_mask:0xf\n"
    "s_nop 1\n"
    "v_max_i32_dpp v209, v209, v209 row_ror:8 row_mask:0xf bank_mask:0xf\n"
    "v_mov_b32 v210, v209\n"
    "s_nop 1\n"
    "v_permlane16_swap_b32 v209, v210\n"
    "v_max_i32 v209, v209, v210\n"
    "v_mov_b32 v210, v209\n"
    "s_nop 1\n"
    "v_permlane32_swap_b32 v209, v210\n"
    "v_max_i32 v209, v209, v210\n"
    "s_nop 0\n"
    "v_readfirstlane_b32 s84, v209\n"
    "s_sub_i32 s85, s84, 121\n"
    "s_and_b32 s85, s85, -4\n"
    "s_cmp_gt_i32 s84, 172\n"
    "s_cselect_b32 s85, s85, 0\n"
    "s_add_u32 s11, s11, s85\n"
    "Lwt_41:\n"
    "s_memrealtime s[78:79]\n"
    "s_waitcnt lgkmcnt(0)\n"
    "s_sub_u32 s78, s78, s11\n"
    "s_cmp_lt_u32 s78, 296\n"
    "s_cbranch_scc1 Lwt_41\n"
    "v_mov_b32 v164, -1\n"
    "v_mov_b32 v165, -1\n"
    "v_mov_b32 v166, -1\n"
    "v_mov_b32 v167, -1\n"
    "s_mov_b32 s84, 0x400000\n"
    "buffer_store_dwordx4 v[164:167], v118, s[12:15], s84 offen offset:0 sc1\n"
    "buffer_store_dwordx4 v[164:167], v118, s[12:15], s84 offen offset:1024 sc1\n"
    "buffer_store_dwordx4 v[164:167], v118, s[12:15], s84 offen offset:2048 sc1\n"
    "buffer_store_dwordx4 v[164:167], v118, s[12:15], s84 offen offset:3072 sc1\n"
    "s_mov_b32 s84, 0x408000\n"
    "buffer_store_dwordx4 v[164:167], v118, s[12:15], s84 offen offset:0 sc1\n"
    "buffer_store_dwordx4 v[164:167], v118, s[12:15], s84 offen offset:1024 sc1\n"
    "buffer_store_dwordx4 v[164:167], v118, s[12:15], s84 offen offset:2048 sc1\n"
    "buffer_store_dwordx4 v[164:167], v118, s[12:15], s84 offen offset:3072 sc1\n"
    "s_lshl_b32 s84, s61, 31\n"
    "v_or_b32 v120, s84, v120\n"
    "v_add_u32 v117, 0x400000, v117\n"
    "v_add_u32 v118, 0x400000, v118\n"
    "s_bfe_u32 s85, s61, 0x20001\n"
    "s_lshl_b32 s84, s85, 10\n"
    "s_add_u32 s84, s84, s68\n"
    "s_add_u32 s87, s84, 0xe8000\n"
    "v_lshl_add_u32 v5, v1, 4, s87\n"
    "s_mov_b32 s66, 0xe8000\n"
    "s_mov_b64 exec, 0\n"
    "s_cmp_eq_u32 s59, 0\n"
    "s_cbranch_scc0 Lwn_43\n"
    "s_call_b64 s[0:1], Lseg_A1_0\n"
    "Lwn_43:\n"
    "s_cmp_eq_u32 s59, 1\n"
    "s_cbranch_scc0 Lwn_44\n"
    "s_call_b64 s[0:1], Lseg_B1_0\n"
    "Lwn_44:\n"
    "s_cmp_eq_u32 s59, 2\n"
    "s_cbranch_scc0 Lwn_45\n"
    "s_call_b64 s[0:1], Lseg_C1_0\n"
    "Lwn_45:\n"
    "s_mov_b64 exec, -1\n"
    "v_bfe_u32 v209, v1, 1, 4\n"
    "v_and_b32 v210, 1, v1\n"
    "s_add_u32 s87, s68, 0x144000\n"
    "v_lshl_add_u32 v208, v209, 7, s87\n"
    "v_lshl_add_u32 v208, v210, 4, v208\n"
    "Lwt_46:\n"
    "s_memrealtime s[78:79]\n"
    "s_waitcnt lgkmcnt(0)\n"
    "s_sub_u32 s78, s78, s11\n"
    "s_cmp_lt_u32 s78, 392\n"
    "s_cbranch_scc1 Lwt_46\n"
    "buffer_load_dwordx4 v[204:207], v208, s[12:15], 0 offen sc1\n"
    "buffer_load_dwordx4 v[168:171], v4, s[12:15], 0 offen sc1\n"
    "Lqcc_50:\n"
    "s_waitcnt vmcnt(0)\n"
    "v_mfma_f32_16x16x32_bf16 v[172:175], v[168:171], v[32:35], 0\n"
    "v_mfma_f32_16x16x32_bf16 v[176:179], v[168:171], v[36:39], 0\n"
    "v_mfma_f32_16x16x32_bf16 v[180:183], v[168:171], v[40:43], 0\n"
    "v_mfma_f32_16x16x32_bf16 v[184:187], v[168:171], v[44:47], 0\n"
    "s_nop 6\n"
    "v_permlane32_swap_b32 v172, v180\n"
    "v_permlane32_swap_b32 v176, v184\n"
    "v_add_f32 v172, v172, v180\n"
    "v_add_f32 v176, v176, v184\n"
    "s_nop 1\n"
    "v_permlane16_swap_b32 v172, v176\n"
    "v_add_f32 v172, v172, v176\n"
    "ds_write_b32 v139, v172 offset:7168\n"
    "v_cmp_u_f32 vcc, v172, v172\n"
    "s_cbranch_vccnz Lqcs_51\n"
    "Lqcd_52:\n"
    "s_waitcnt lgkmcnt(0)\n"
    "s_barrier\n"
    "ds_read2st64_b32 v[168:169], v140 offset0:28 offset1:32\n"
    "ds_read2st64_b32 v[170:171], v140 offset0:36 offset1:40\n"
    "s_waitcnt lgkmcnt(0)\n"
    "v_pk_add_f32 v[168:169], v[168:169], v[170:171]\n"
    "v_add_f32 v4, v168, v169\n"
    "s_barrier\n"
    "Ladc_47:\n"
    "v_max3_u32 v209, v204, v205, v206\n"
    "v_max_u32 v209, v209, v207\n"
    "v_cmp_eq_u32 vcc, -1, v209\n"
    "s_cbranch_vccnz Lads_48\n"
    "Ladd_49:\n"
    "v_subrev_u32 v204, s11, v204\n"
    "v_subrev_u32 v205, s11, v205\n"
    "v_subrev_u32 v206, s11, v206\n"
    "v_subrev_u32 v207, s11, v207\n"
    "v_max3_i32 v209, v204, v205, v206\n"
    "v_max_i32 v209, v209, v207\n"
    "s_nop 1\n"
    "v_max_i32_dpp v209, v209, v209 row_ror:1 row_mask:0xf bank_mask:0xf\n"
    "s_nop 1\n"
    "v_max_i32_dpp v209, v209, v209 row_ror:2 row_mask:0xf bank_mask:0xf\n"
    "s_nop 1\n"
    "v_max_i32_dpp v209, v209, v209 row_ror:4 row_mask:0xf bank_mask:0xf\n"
    "s_nop 1\n"
    "v_max_i32_dpp v209, v209, v209 row_ror:8 row_mask:0xf bank_mask:0xf\n"
    "v_mov_b32 v210, v209\n"
    "s_nop 1\n"
    "v_permlane16_swap_b32 v209, v210\n"
    "v_max_i32 v209, v209, v210\n"
    "v_mov_b32 v210, v209\n"
    "s_nop 1\n"
    "v_permlane32_swap_b32 v209, v210\n"
    "v_max_i32 v209, v209, v210\n"
    "s_nop 0\n"
    "v_readfirstlane_b32 s84, v209\n"
    "s_sub_i32 s85, s84, 217\n"
    "s_and_b32 s85, s85, -4\n"
    "s_cmp_gt_i32 s84, 280\n"
    "s_cselect_b32 s85, s85, 0\n"
    "s_add_u32 s11, s11, s85\n"
    "s_branch Ldj_25\n"
    "Lfc_23:\n"
    "s_mov_b32 m0, 156928\n"
    "s_nop 0\n"
    "buffer_load_dword v246, s[40:43], 0 offen lds\n"
    "s_mov_b32 m0, 156928\n"
    "s_nop 0\n"
    "buffer_load_dword v246, s[44:47], 0 offen lds\n"
    "s_cmp_ge_u32 s97, 14\n"
    "s_cbranch_scc1 Ld2_24\n"
    "s_mul_i32 s96, s57, 14\n"
    "s_add_u32 s96, s96, s97\n"
    "buffer_load_dwordx4 v[12:15], v242, s[20:23], 0 offen\n"
    "s_mov_b64 exec, 0\n"
    "s_cmp_eq_u32 s59, 0\n"
    "s_cbranch_scc0 Lwn_54\n"
    "s_call_b64 s[0:1], Lseg_D10_0\n"
    "Lwn_54:\n"
    "s_cmp_eq_u32 s59, 1\n"
    "s_cbranch_scc0 Lwn_55\n"
    "s_call_b64 s[0:1], Lseg_A1_0\n"
    "Lwn_55:\n"
    "s_cmp_eq_u32 s59, 2\n"
    "s_cbranch_scc0 Lwn_56\n"
    "s_call_b64 s[0:1], Lseg_B1_0\n"
    "Lwn_56:\n"
    "s_cmp_eq_u32 s59, 3\n"
    "s_cbranch_scc0 Lwn_57\n"
    "s_call_b64 s[0:1], Lseg_D11_0\n"
    "Lwn_57:\n"
    "s_mov_b64 exec, -1\n"
    "s_add_u32 s88, s83, 0x20000\n"
    "s_add_u32 s89, s83, 0x21000\n"
    "s_add_u32 s90, s83, 0x24000\n"
    "s_add_u32 s91, s83, 0x25000\n"
    "s_add_u32 s92, s83, 0x28000\n"
    "s_add_u32 s93, s83, 0x29000\n"
    "s_add_u32 s94, s83, 0x2c000\n"
    "s_add_u32 s95, s83, 0x2d000\n"
    "v_mov_b32 v188, 0x3f803f80\n"
    "v_mov_b32 v189, 0x3f803f80\n"
    "v_mov_b32 v190, 0x3f803f80\n"
    "v_mov_b32 v191, 0x3f803f80\n"
    "v_mov_b32 v192, 0x3f803f80\n"
    "v_mov_b32 v193, 0x3f803f80\n"
    "v_mov_b32 v194, 0x3f803f80\n"
    "v_mov_b32 v195, 0x3f803f80\n"
    "v_mov_b32 v196, 0x3f803f80\n"
    "v_mov_b32 v197, 0x3f803f80\n"
    "v_mov_b32 v198, 0x3f803f80\n"
    "v_mov_b32 v199, 0x3f803f80\n"
    "v_mov_b32 v200, 0x3f803f80\n"
    "v_mov_b32 v201, 0x3f803f80\n"
    "v_mov_b32 v202, 0x3f803f80\n"
    "v_mov_b32 v203, 0x3f803f80\n"
    "s_mul_i32 s84, s59, 1280\n"
    "s_add_u32 s87, s84, 0x21000\n"
    "v_mul_u32_u24 v230, 288, v3\n"
    "v_add_u32 v227, s87, v230\n"
    "v_lshl_add_u32 v227, v2, 4, v227\n"
    "s_mov_b32 s87, 0x21000\n"
    "v_mul_u32_u24 v230, 288, v9\n"
    "v_add_u32 v224, s87, v230\n"
    "v_lshl_add_u32 v224, v3, 4, v224\n"
    "v_lshl_add_u32 v224, v10, 2, v224\n"
    "s_mov_b32 s87, 0x21040\n"
    "v_mul_u32_u24 v230, 288, v9\n"
    "v_add_u32 v228, s87, v230\n"
    "v_lshl_add_u32 v228, v10, 2, v228\n"
    "s_mov_b32 s87, 0x21000\n"
    "v_mul_u32_u24 v230, 20, v3\n"
    "v_add_u32 v225, s87, v230\n"
    "s_mov_b32 s87, 0x21040\n"
    "v_lshl_add_u32 v226, v3, 2, s87\n"
    "s_bfe_u32 s85, s96, 0x10006\n"
    "s_lshl_b32 s84, s85, 14\n"
    "s_bfe_u32 s85, s96, 0x60000\n"
    "s_lshl_b32 s86, s85, 7\n"
    "s_add_u32 s84, s84, s86\n"
    "s_add_u32 s84, s84, s83\n"
    "s_add_u32 s6, s84, 0x30000\n"
    "s_lshl_b32 s84, s97, 7\n"
    "s_lshl_b32 s86, s57, 2\n"
    "s_add_u32 s84, s84, s86\n"
    "s_add_u32 s87, s84, 0x144000\n"
    "v_lshl_add_u32 v248, v0, 13, s87\n"
    "v_mov_b32 v249, 0x80000000\n"
    "v_cmp_gt_u32 vcc, 2, v0\n"
    "s_nop 1\n"
    "v_cndmask_b32 v248, v249, v248, vcc\n"
    "s_mul_i32 s96, s57, 14\n"
    "s_add_u32 s96, s96, s97\n"
    "v_lshrrev_b32 v220, 1, v1\n"
    "v_and_b32 v221, 1, v1\n"
    "s_mul_i32 s84, s59, 458752\n"
    "s_lshl_b32 s86, s96, 5\n"
    "s_add_u32 s84, s84, s86\n"
    "s_cmp_lt_u32 s97, 14\n"
    "s_cselect_b32 s84, s84, 0x80000000\n"
    "v_mul_u32_u24 v230, 3584, v220\n"
    "v_add_u32 v238, s84, v230\n"
    "v_lshl_add_u32 v238, v221, 4, v238\n"
    "s_lshl_b32 s5, s59, 13\n"
    "s_add_u32 s85, s96, 0\n"
    "s_and_b32 s85, s85, 3\n"
    "s_mul_i32 s87, s85, 114688\n"
    "s_lshl_b32 s85, s85, 10\n"
    "s_add_u32 s85, s85, s5\n"
    "s_add_u32 m0, s85, 0\n"
    "s_nop 0\n"
    "buffer_load_dwordx4 v238, s[40:43], s87 offen offset:0 lds\n"
    "s_add_u32 m0, s85, 4096\n"
    "s_nop 0\n"
    "buffer_load_dwordx4 v238, s[44:47], s87 offen offset:0 lds\n"
    "s_add_u32 s85, s96, 1\n"
    "s_and_b32 s85, s85, 3\n"
    "s_mul_i32 s87, s85, 114688\n"
    "s_lshl_b32 s85, s85, 10\n"
    "s_add_u32 s85, s85, s5\n"
    "s_add_u32 m0, s85, 0\n"
    "s_nop 0\n"
    "buffer_load_dwordx4 v238, s[40:43], s87 offen offset:0 lds\n"
    "s_add_u32 m0, s85, 4096\n"
    "s_nop 0\n"
    "buffer_load_dwordx4 v238, s[44:47], s87 offen offset:0 lds\n"
    "s_add_u32 s85, s96, 2\n"
    "s_and_b32 s85, s85, 3\n"
    "s_mul_i32 s87, s85, 114688\n"
    "s_lshl_b32 s85, s85, 10\n"
    "s_add_u32 s85, s85, s5\n"
    "s_add_u32 m0, s85, 0\n"
    "s_nop 0\n"
    "buffer_load_dwordx4 v238, s[40:43], s87 offen offset:0 lds\n"
    "s_add_u32 m0, s85, 4096\n"
    "s_nop 0\n"
    "buffer_load_dwordx4 v238, s[44:47], s87 offen offset:0 lds\n"
    "s_add_u32 s85, s96, 3\n"
    "s_and_b32 s85, s85, 3\n"
    "s_mul_i32 s87, s85, 114688\n"
    "s_lshl_b32 s85, s85, 10\n"
    "s_add_u32 s85, s85, s5\n"
    "s_add_u32 m0, s85, 0\n"
    "s_nop 0\n"
    "buffer_load_dwordx4 v238, s[40:43], s87 offen offset:0 lds\n"
    "s_add_u32 m0, s85, 4096\n"
    "s_nop 0\n"
    "buffer_load_dwordx4 v238, s[44:47], s87 offen offset:0 lds\n"
    "buffer_load_dwordx4 v[232:235], v236, s[12:15], 0 offen sc1\n"
    "v_lshl_add_u32 v223, v3, 10, s5\n"
    "v_lshl_add_u32 v223, v9, 5, v223\n"
    "v_lshl_add_u32 v223, v10, 3, v223\n"
    "s_waitcnt vmcnt(1)\n"
    "Lseg_D10_0:\n"
    "s_cbranch_execz Lseg_D10_1\n"
    "ds_read_b64_tr_b16 v[48:49], v223 offset:0\n"
    "ds_read_b64_tr_b16 v[50:51], v223 offset:128\n"
    "ds_read_b64_tr_b16 v[52:53], v223 offset:256\n"
    "ds_read_b64_tr_b16 v[54:55], v223 offset:384\n"
    "ds_read_b64_tr_b16 v[56:57], v223 offset:512\n"
    "ds_read_b64_tr_b16 v[58:59], v223 offset:640\n"
    "ds_read_b64_tr_b16 v[60:61], v223 offset:768\n"
    "ds_read_b64_tr_b16 v[62:63], v223 offset:896\n"
    "ds_read_b64_tr_b16 v[64:65], v223 offset:4096\n"
    "ds_read_b64_tr_b16 v[66:67], v223 offset:4224\n"
    "ds_read_b64_tr_b16 v[68:69], v223 offset:4352\n"
    "ds_read_b64_tr_b16 v[70:71], v223 offset:4480\n"
    "ds_read_b64_tr_b16 v[72:73], v223 offset:4608\n"
    "ds_read_b64_tr_b16 v[74:75], v223 offset:4736\n"
    "ds_read_b64_tr_b16 v[76:77], v223 offset:4864\n"
    "ds_read_b64_tr_b16 v[78:79], v223 offset:4992\n"
    "Lbcc_58:\n"
    "s_waitcnt vmcnt(0)\n"
    "v_max3_u32 v230, v232, v233, v234\n"
    "v_max_u32 v230, v230, v235\n"
    "v_cmp_eq_u32 vcc, -1, v230\n"
    "s_cbranch_vccnz Lbcs_59\n"
    "Lbcd_60:\n"
    "v_mov_b32 v152, 0x3f803f80\n"
    "v_mov_b32 v153, 0x3f803f80\n"
    "v_mov_b32 v154, 0x3f803f80\n"
    "v_mov_b32 v155, 0x3f803f80\n"
    "s_nop 1\n"
    "v_mfma_f32_16x16x32_bf16 v[156:159], v[16:19], v[152:155], 0\n"
    "v_mfma_f32_16x16x32_bf16 v[156:159], v[20:23], v[152:155], v[156:159]\n"
    "v_mfma_f32_16x16x32_bf16 v[156:159], v[24:27], v[152:155], v[156:159]\n"
    "v_mfma_f32_16x16x32_bf16 v[156:159], v[28:31], v[152:155], v[156:159]\n"
    "v_subrev_u32 v232, s11, v232\n"
    "v_subrev_u32 v233, s11, v233\n"
    "v_subrev_u32 v234, s11, v234\n"
    "v_subrev_u32 v235, s11, v235\n"
    "v_max3_i32 v168, v232, v233, v234\n"
    "v_max_i32 v168, v168, v235\n"
    "s_nop 1\n"
    "v_max_i32_dpp v168, v168, v168 row_ror:1 row_mask:0xf bank_mask:0xf\n"
    "s_nop 1\n"
    "v_max_i32_dpp v168, v168, v168 row_ror:2 row_mask:0xf bank_mask:0xf\n"
    "s_nop 1\n"
    "v_max_i32_dpp v168, v168, v168 row_ror:4 row_mask:0xf bank_mask:0xf\n"
    "s_nop 1\n"
    "v_max_i32_dpp v168, v168, v168 row_ror:8 row_mask:0xf bank_mask:0xf\n"
    "v_mov_b32 v169, v168\n"
    "s_nop 1\n"
    "v_permlane16_swap_b32 v168, v169\n"
    "v_max_i32 v168, v168, v169\n"
    "v_mov_b32 v169, v168\n"
    "s_nop 1\n"
    "v_permlane32_swap_b32 v168, v169\n"
    "v_max_i32 v168, v168, v169\n"
    "s_nop 0\n"
    "v_readfirstlane_b32 s84, v168\n"
    "s_add_u32 s11, s11, s84\n"
    "v_bfe_u32 v149, v1, 1, 4\n"
    "v_and_b32 v150, 1, v1\n"
    "s_add_u32 s87, s68, 0x148000\n"
    "v_lshl_add_u32 v148, v149, 7, s87\n"
    "v_lshl_add_u32 v148, v150, 4, v148\n"
    "s_waitcnt lgkmcnt(0)\n"
    "Lwt_61:\n"
    "s_memrealtime s[78:79]\n"
    "s_waitcnt lgkmcnt(0)\n"
    "s_sub_u32 s78, s78, s11\n"
    "s_cmp_lt_u32 s78, 192\n"
    "s_cbranch_scc1 Lwt_61\n"
    "buffer_load_dwordx4 v[144:147], v148, s[12:15], 0 offen sc1\n"
    "v_cvt_pk_bf16_f32 v164, v156, v157\n"
    "v_cvt_pk_bf16_f32 v165, v158, v159\n"
    "s_mov_b32 s84, 0x50000\n"
    "buffer_store_dwordx2 v[164:165], v112, s[12:15], s84 offen sc1\n"
    "v_add_u32 v4, s84, v113\n"
    "v_mov_b32 v170, -1\n"
    "v_mov_b32 v171, -1\n"
    "v_mov_b32 v172, -1\n"
    "v_mov_b32 v173, -1\n"
    "s_mul_i32 s84, s57, 73728\n"
    "s_mul_i32 s86, s97, 4608\n"
    "s_add_u32 s84, s84, s86\n"
    "v_lshl_add_u32 v174, v0, 4, s84\n"
    "buffer_store_dwordx4 v[170:173], v174, s[12:15], 0 offen sc1\n"
    "buffer_store_dwordx4 v[170:173], v174, s[12:15], 0 offen offset:512 sc1\n"
    "Lac0c_62:\n"
    "s_waitcnt vmcnt(3)\n"
    "v_max3_u32 v149, v144, v145, v146\n"
    "v_max_u32 v149, v149, v147\n"
    "v_cmp_eq_u32 vcc, -1, v149\n"
    "s_cbranch_vccnz Lac0s_63\n"
    "Lac0d_64:\n"
    "v_subrev_u32 v144, s11, v144\n"
    "v_subrev_u32 v145, s11, v145\n"
    "v_subrev_u32 v146, s11, v146\n"
    "v_subrev_u32 v147, s11, v147\n"
    "v_max3_i32 v149, v144, v145, v146\n"
    "v_max_i32 v149, v149, v147\n"
    "s_nop 1\n"
    "v_max_i32_dpp v149, v149, v149 row_ror:1 row_mask:0xf bank_mask:0xf\n"
    "s_nop 1\n"
    "v_max_i32_dpp v149, v149, v149 row_ror:2 row_mask:0xf bank_mask:0xf\n"
    "s_nop 1\n"
    "v_max_i32_dpp v149, v149, v149 row_ror:4 row_mask:0xf bank_mask:0xf\n"
    "s_nop 1\n"
    "v_max_i32_dpp v149, v149, v149 row_ror:8 row_mask:0xf bank_mask:0xf\n"
    "v_mov_b32 v150, v149\n"
    "s_nop 1\n"
    "v_permlane16_swap_b32 v149, v150\n"
    "v_max_i32 v149, v149, v150\n"
    "v_mov_b32 v150, v149\n"
    "s_nop 1\n"
    "v_permlane32_swap_b32 v149, v150\n"
    "v_max_i32 v149, v149, v150\n"
    "s_nop 0\n"
    "v_readfirstlane_b32 s84, v149\n"
    "s_sub_i32 s85, s84, 121\n"
    "s_and_b32 s85, s85, -4\n"
    "s_cmp_gt_i32 s84, 172\n"
    "s_cselect_b32 s85, s85, 0\n"
    "s_add_u32 s11, s11, s85\n"
    "Lwt_65:\n"
    "s_memrealtime s[78:79]\n"
    "s_waitcnt lgkmcnt(0)\n"
    "s_sub_u32 s78, s78, s11\n"
    "s_cmp_lt_u32 s78, 272\n"
    "s_cbranch_scc1 Lwt_65\n"
    "s_add_u32 s80, s78, s11\n"
    "buffer_load_dwordx4 v[144:147], v122, s[12:15], s88 offen sc1\n"
    "s_nop 11\n"
    "buffer_load_dwordx4 v[148:151], v122, s[12:15], s89 offen sc1\n"
    "s_nop 11\n"
    "buffer_load_dwordx4 v[152:155], v122, s[12:15], s90 offen sc1\n"
    "s_nop 11\n"
    "buffer_load_dwordx4 v[156:159], v122, s[12:15], s91 offen sc1\n"
    "s_nop 11\n"
    "buffer_load_dwordx4 v[160:163], v122, s[12:15], s92 offen sc1\n"
    "s_nop 11\n"
    "buffer_load_dwordx4 v[164:167], v122, s[12:15], s93 offen sc1\n"
    "s_nop 11\n"
    "buffer_load_dwordx4 v[168:171], v122, s[12:15], s94 offen sc1\n"
    "s_nop 11\n"
    "buffer_load_dwordx4 v[172:175], v122, s[12:15], s95 offen sc1\n"
    "Lh40c_66:\n"
    "v_lshlrev_b32 v176, 16, v12\n"
    "v_and_b32 v177, 0xffff0000, v12\n"
    "v_lshlrev_b32 v178, 16, v13\n"
    "Lseg_D10_1:\n"
    "s_cbranch_execz Lseg_D10_2\n"
    "v_and_b32 v179, 0xffff0000, v13\n"
    "v_lshlrev_b32 v180, 16, v14\n"
    "v_and_b32 v181, 0xffff0000, v14\n"
    "v_lshlrev_b32 v182, 16, v15\n"
    "v_and_b32 v183, 0xffff0000, v15\n"
    "s_waitcnt vmcnt(7)\n"
    "v_lshlrev_b32 v216, 16, v144\n"
    "v_and_b32 v217, 0xffff0000, v144\n"
    "v_lshlrev_b32 v218, 16, v145\n"
    "v_and_b32 v219, 0xffff0000, v145\n"
    "v_pk_add_f32 v[176:177], v[176:177], v[216:217]\n"
    "v_lshlrev_b32 v220, 16, v146\n"
    "v_and_b32 v221, 0xffff0000, v146\n"
    "v_pk_add_f32 v[178:179], v[178:179], v[218:219]\n"
    "v_lshlrev_b32 v222, 16, v147\n"
    "v_and_b32 v223, 0xffff0000, v147\n"
    "v_pk_add_f32 v[180:181], v[180:181], v[220:221]\n"
    "v_pk_add_f32 v[182:183], v[182:183], v[222:223]\n"
    "s_waitcnt vmcnt(6)\n"
    "v_lshlrev_b32 v216, 16, v148\n"
    "v_and_b32 v217, 0xffff0000, v148\n"
    "v_lshlrev_b32 v218, 16, v149\n"
    "v_and_b32 v219, 0xffff0000, v149\n"
    "v_pk_add_f32 v[176:177], v[176:177], v[216:217]\n"
    "v_lshlrev_b32 v220, 16, v150\n"
    "v_and_b32 v221, 0xffff0000, v150\n"
    "v_pk_add_f32 v[178:179], v[178:179], v[218:219]\n"
    "v_lshlrev_b32 v222, 16, v151\n"
    "v_and_b32 v223, 0xffff0000, v151\n"
    "v_pk_add_f32 v[180:181], v[180:181], v[220:221]\n"
    "v_pk_add_f32 v[182:183], v[182:183], v[222:223]\n"
    "s_waitcnt vmcnt(5)\n"
    "v_lshlrev_b32 v216, 16, v152\n"
    "v_and_b32 v217, 0xffff0000, v152\n"
    "v_lshlrev_b32 v218, 16, v153\n"
    "v_and_b32 v219, 0xffff0000, v153\n"
    "v_pk_add_f32 v[176:177], v[176:177], v[216:217]\n"
    "v_lshlrev_b32 v220, 16, v154\n"
    "v_and_b32 v221, 0xffff0000, v154\n"
    "v_pk_add_f32 v[178:179], v[178:179], v[218:219]\n"
    "v_lshlrev_b32 v222, 16, v155\n"
    "v_and_b32 v223, 0xffff0000, v155\n"
    "v_pk_add_f32 v[180:181], v[180:181], v[220:221]\n"
    "v_pk_add_f32 v[182:183], v[182:183], v[222:223]\n"
    "s_waitcnt vmcnt(4)\n"
    "v_lshlrev_b32 v216, 16, v156\n"
    "v_and_b32 v217, 0xffff0000, v156\n"
    "v_lshlrev_b32 v218, 16, v157\n"
    "v_and_b32 v219, 0xffff0000, v157\n"
    "v_pk_add_f32 v[176:177], v[176:177], v[216:217]\n"
    "v_lshlrev_b32 v220, 16, v158\n"
    "v_and_b32 v221, 0xffff0000, v158\n"
    "v_pk_add_f32 v[178:179], v[178:179], v[218:219]\n"
    "v_lshlrev_b32 v222, 16, v159\n"
    "v_and_b32 v223, 0xffff0000, v159\n"
    "v_pk_add_f32 v[180:181], v[180:181], v[220:221]\n"
    "v_pk_add_f32 v[182:183], v[182:183], v[222:223]\n"
    "s_waitcnt vmcnt(3)\n"
    "v_lshlrev_b32 v216, 16, v160\n"
    "v_and_b32 v217, 0xffff0000, v160\n"
    "v_lshlrev_b32 v218, 16, v161\n"
    "v_and_b32 v219, 0xffff0000, v161\n"
    "v_pk_add_f32 v[176:177], v[176:177], v[216:217]\n"
    "v_lshlrev_b32 v220, 16, v162\n"
    "v_and_b32 v221, 0xffff0000, v162\n"
    "v_pk_add_f32 v[178:179], v[178:179], v[218:219]\n"
    "v_lshlrev_b32 v222, 16, v163\n"
    "v_and_b32 v223, 0xffff0000, v163\n"
    "v_pk_add_f32 v[180:181], v[180:181], v[220:221]\n"
    "v_pk_add_f32 v[182:183], v[182:183], v[222:223]\n"
    "s_waitcnt vmcnt(2)\n"
    "v_lshlrev_b32 v216, 16, v164\n"
    "v_and_b32 v217, 0xffff0000, v164\n"
    "v_lshlrev_b32 v218, 16, v165\n"
    "v_and_b32 v219, 0xffff0000, v165\n"
    "v_pk_add_f32 v[176:177], v[176:177], v[216:217]\n"
    "v_lshlrev_b32 v220, 16, v166\n"
    "v_and_b32 v221, 0xffff0000, v166\n"
    "v_pk_add_f32 v[178:179], v[178:179], v[218:219]\n"
    "v_lshlrev_b32 v222, 16, v167\n"
    "v_and_b32 v223, 0xffff0000, v167\n"
    "v_pk_add_f32 v[180:181], v[180:181], v[220:221]\n"
    "v_pk_add_f32 v[182:183], v[182:183], v[222:223]\n"
    "s_waitcnt vmcnt(1)\n"
    "v_lshlrev_b32 v216, 16, v168\n"
    "v_and_b32 v217, 0xffff0000, v168\n"
    "v_lshlrev_b32 v218, 16, v169\n"
    "v_and_b32 v219, 0xffff0000, v169\n"
    "v_pk_add_f32 v[176:177], v[176:177], v[216:217]\n"
    "v_lshlrev_b32 v220, 16, v170\n"
    "v_and_b32 v221, 0xffff0000, v170\n"
    "v_pk_add_f32 v[178:179], v[178:179], v[218:219]\n"
    "v_lshlrev_b32 v222, 16, v171\n"
    "v_and_b32 v223, 0xffff0000, v171\n"
    "v_pk_add_f32 v[180:181], v[180:181], v[220:221]\n"
    "v_pk_add_f32 v[182:183], v[182:183], v[222:223]\n"
    "s_waitcnt vmcnt(0)\n"
    "v_lshlrev_b32 v216, 16, v172\n"
    "v_and_b32 v217, 0xffff0000, v172\n"
    "v_lshlrev_b32 v218, 16, v173\n"
    "v_and_b32 v219, 0xffff0000, v173\n"
    "v_pk_add_f32 v[176:177], v[176:177], v[216:217]\n"
    "v_lshlrev_b32 v220, 16, v174\n"
    "v_and_b32 v221, 0xffff0000, v174\n"
    "v_pk_add_f32 v[178:179], v[178:179], v[218:219]\n"
    "v_lshlrev_b32 v222, 16, v175\n"
    "v_and_b32 v223, 0xffff0000, v175\n"
    "v_pk_add_f32 v[180:181], v[180:181], v[220:221]\n"
    "v_pk_add_f32 v[182:183], v[182:183], v[222:223]\n"
    "v_cvt_pk_bf16_f32 v184, v176, v177\n"
    "v_cvt_pk_bf16_f32 v185, v178, v179\n"
    "v_cvt_pk_bf16_f32 v186, v180, v181\n"
    "v_cvt_pk_bf16_f32 v187, v182, v183\n"
    "v_mov_b32_dpp v188, v184 quad_perm:[0,1,2,3] row_mask:0xf bank_mask:0x1\n"
    "v_mov_b32_dpp v189, v185 quad_perm:[0,1,2,3] row_mask:0xf bank_mask:0x1\n"
    "v_mov_b32_dpp v190, v186 quad_perm:[0,1,2,3] row_mask:0xf bank_mask:0x1\n"
    "v_mov_b32_dpp v191, v187 quad_perm:[0,1,2,3] row_mask:0xf bank_mask:0x1\n"
    "v_mov_b32_dpp v192, v184 row_shl:4 row_mask:0xf bank_mask:0x1\n"
    "v_mov_b32_dpp v193, v185 row_shl:4 row_mask:0xf bank_mask:0x1\n"
    "v_mov_b32_dpp v194, v186 row_shl:4 row_mask:0xf bank_mask:0x1\n"
    "v_mov_b32_dpp v195, v187 row_shl:4 row_mask:0xf bank_mask:0x1\n"
    "v_mfma_f32_16x16x32_bf16 v[212:215], v[188:191], v[188:191], 0\n"
    "v_mfma_f32_16x16x32_bf16 v[204:207], v[48:51], v[188:191], 0\n"
    "v_mfma_f32_16x16x32_bf16 v[208:211], v[64:67], v[188:191], 0\n"
    "v_mov_b32_dpp v196, v184 row_shl:8 row_mask:0xf bank_mask:0x1\n"
    "v_mov_b32_dpp v197, v185 row_shl:8 row_mask:0xf bank_mask:0x1\n"
    "v_mov_b32_dpp v198, v186 row_shl:8 row_mask:0xf bank_mask:0x1\n"
    "v_mov_b32_dpp v199, v187 row_shl:8 row_mask:0xf bank_mask:0x1\n"
    "v_mfma_f32_16x16x32_bf16 v[212:215], v[192:195], v[192:195], v[212:215]\n"
    "v_mfma_f32_16x16x32_bf16 v[204:207], v[52:55], v[192:195], v[204:207]\n"
    "v_mfma_f32_16x16x32_bf16 v[208:211], v[68:71], v[192:195], v[208:211]\n"
    "v_mov_b32_dpp v200, v184 row_shl:12 row_mask:0xf bank_mask:0x1\n"
    "v_mov_b32_dpp v201, v185 row_shl:12 row_mask:0xf bank_mask:0x1\n"
    "v_mov_b32_dpp v202, v186 row_shl:12 row_mask:0xf bank_mask:0x1\n"
    "v_mov_b32_dpp v203, v187 row_shl:12 row_mask:0xf bank_mask:0x1\n"
    "v_mfma_f32_16x16x32_bf16 v[212:215], v[196:199], v[196:199], v[212:215]\n"
    "v_mfma_f32_16x16x32_bf16 v[204:207], v[56:59], v[196:199], v[204:207]\n"
    "v_mfma_f32_16x16x32_bf16 v[208:211], v[72:75], v[196:199], v[208:211]\n"
    "v_mfma_f32_16x16x32_bf16 v[212:215], v[200:203], v[200:203], v[212:215]\n"
    "v_mfma_f32_16x16x32_bf16 v[204:207], v[60:63], v[200:203], v[204:207]\n"
    "v_mfma_f32_16x16x32_bf16 v[208:211], v[76:79], v[200:203], v[208:211]\n"
    "s_min_u32 s80, s80, 0xfffffffe\n"
    "v_mov_b32 v249, s80\n"
    "buffer_store_dword v249, v248, s[12:15], 0 offen sc1\n"
    "s_nop 2\n"
    "ds_write_b128 v227, v[212:215] offset:10240\n"
    "ds_write_b128 v227, v[204:207] offset:0\n"
    "ds_write_b128 v227, v[208:211] offset:5120\n"
    "v_cmp_u_f32 vcc, v204, v204\n"
    "s_cbranch_vccnz Lh40s_67\n"
    "Lh40d_68:\n"
    "s_cmp_lt_u32 s59, 1\n"
    "s_waitcnt lgkmcnt(0)\n"
    "Lseg_D10_2:\n"
    "s_cbranch_execz Ltret\n"
    "s_barrier\n"
    "s_cbranch_scc0 Lsk_69\n"
    "ds_read2st64_b32 v[144:145], v225 offset0:40 offset1:45\n"
    "ds_read2st64_b32 v[146:147], v225 offset0:50 offset1:55\n"
    "ds_read2st64_b32 v[148:149], v226 offset0:40 offset1:45\n"
    "ds_read2st64_b32 v[150:151], v226 offset0:50 offset1:55\n"
    "ds_read2st64_b32 v[152:153], v224 offset0:0 offset1:5\n"
    "ds_read2st64_b32 v[154:155], v224 offset0:10 offset1:15\n"
    "ds_read2st64_b32 v[160:161], v224 offset0:20 offset1:25\n"
    "ds_read2st64_b32 v[162:163], v224 offset0:30 offset1:35\n"
    "ds_read2st64_b32 v[156:157], v228 offset0:0 offset1:5\n"
    "ds_read2st64_b32 v[158:159], v228 offset0:10 offset1:15\n"
    "ds_read2st64_b32 v[164:165], v228 offset0:20 offset1:25\n"
    "ds_read2st64_b32 v[166:167], v228 offset0:30 offset1:35\n"
    "s_waitcnt lgkmcnt(10)\n"
    "v_pk_add_f32 v[144:145], v[144:145], v[146:147]\n"
    "s_waitcnt lgkmcnt(8)\n"
    "v_pk_add_f32 v[148:149], v[148:149], v[150:151]\n"
    "v_add_f32 v144, v144, v145\n"
    "v_add_f32 v148, v148, v149\n"
    "v_mul_f32 v168, 0x3b000000, v148\n"
    "v_mul_f32 v170, 0x3b000000, v144\n"
    "v_fma_f32 v170, -v168, v168, v170\n"
    "s_waitcnt lgkmcnt(6)\n"
    "v_pk_add_f32 v[152:153], v[152:153], v[154:155]\n"
    "v_max_f32 v170, 0, v170\n"
    "v_add_f32 v152, v152, v153\n"
    "v_add_f32 v170, 0x3727c5ac, v170\n"
    "s_waitcnt lgkmcnt(2)\n"
    "v_pk_add_f32 v[156:157], v[156:157], v[158:159]\n"
    "s_waitcnt lgkmcnt(0)\n"
    "v_pk_add_f32 v[164:165], v[164:165], v[166:167]\n"
    "v_add_f32 v80, v156, v157\n"
    "v_add_f32 v81, v164, v165\n"
    "v_rsq_f32 v169, v170\n"
    "v_fma_f32 v152, -v168, v80, v152\n"
    "v_mul_f32 v153, 0xbfb8aa3b, v152\n"
    "v_mul_f32 v153, v169, v153\n"
    "v_pk_add_f32 v[160:161], v[160:161], v[162:163]\n"
    "v_exp_f32 v153, v153\n"
    "v_add_f32 v160, v160, v161\n"
    "v_add_f32 v153, 1.0, v153\n"
    "v_fma_f32 v160, -v168, v81, v160\n"
    "v_rcp_f32 v153, v153\n"
    "v_mul_f32 v152, v169, v152\n"
    "v_mul_f32 v160, v169, v160\n"
    "v_mul_f32 v152, v152, v153\n"
    "v_mul_f32 v152, v152, v160\n"
    "s_nop 1\n"
    "v_mov_b32_dpp v153, v152 quad_perm:[0,0,2,2] row_mask:0xf bank_mask:0xf\n"
    "v_mov_b32_dpp v154, v152 quad_perm:[1,1,3,3] row_mask:0xf bank_mask:0xf\n"
    "v_cvt_pk_bf16_f32 v153, v153, v154\n"
    "buffer_store_dword v153, v121, s[12:15], s6 offen sc1\n"
    "Lsk_69:\n"
    "s_nop 0\n"
    "v_bfe_u32 v209, v1, 1, 4\n"
    "v_and_b32 v210, 1, v1\n"
    "s_add_u32 s87, s68, 0x144000\n"
    "v_lshl_add_u32 v208, v209, 7, s87\n"
    "v_lshl_add_u32 v208, v210, 4, v208\n"
    "Lwt_70:\n"
    "s_memrealtime s[78:79]\n"
    "s_waitcnt lgkmcnt(0)\n"
    "s_sub_u32 s78, s78, s11\n"
    "s_cmp_lt_u32 s78, 392\n"
    "s_cbranch_scc1 Lwt_70\n"
    "buffer_load_dwordx4 v[204:207], v208, s[12:15], 0 offen sc1\n"
    "buffer_load_dwordx4 v[168:171], v4, s[12:15], 0 offen sc1\n"
    "Lqcc_74:\n"
    "s_waitcnt vmcnt(0)\n"
    "v_mfma_f32_16x16x32_bf16 v[172:175], v[168:171], v[32:35], 0\n"
    "v_mfma_f32_16x16x32_bf16 v[176:179], v[168:171], v[36:39], 0\n"
    "v_mfma_f32_16x16x32_bf16 v[180:183], v[168:171], v[40:43], 0\n"
    "v_mfma_f32_16x16x32_bf16 v[184:187], v[168:171], v[44:47], 0\n"
    "s_nop 6\n"
    "v_permlane32_swap_b32 v172, v180\n"
    "v_permlane32_swap_b32 v176, v184\n"
    "v_add_f32 v172, v172, v180\n"
    "v_add_f32 v176, v176, v184\n"
    "s_nop 1\n"
    "v_permlane16_swap_b32 v172, v176\n"
    "v_add_f32 v172, v172, v176\n"
    "ds_write_b32 v139, v172 offset:7168\n"
    "v_cmp_u_f32 vcc, v172, v172\n"
    "s_cbranch_vccnz Lqcs_75\n"
    "Lqcd_76:\n"
    "s_waitcnt lgkmcnt(0)\n"
    "s_barrier\n"
    "ds_read2st64_b32 v[168:169], v140 offset0:28 offset1:32\n"
    "ds_read2st64_b32 v[170:171], v140 offset0:36 offset1:40\n"
    "s_waitcnt lgkmcnt(0)\n"
    "v_pk_add_f32 v[168:169], v[168:169], v[170:171]\n"
    "v_add_f32 v4, v168, v169\n"
    "s_barrier\n"
    "Ladc_71:\n"
    "v_max3_u32 v209, v204, v205, v206\n"
    "v_max_u32 v209, v209, v207\n"
    "v_cmp_eq_u32 vcc, -1, v209\n"
    "s_cbranch_vccnz Lads_72\n"
    "Ladd_73:\n"
    "v_subrev_u32 v204, s11, v204\n"
    "v_subrev_u32 v205, s11, v205\n"
    "v_subrev_u32 v206, s11, v206\n"
    "v_subrev_u32 v207, s11, v207\n"
    "v_max3_i32 v209, v204, v205, v206\n"
    "v_max_i32 v209, v209, v207\n"
    "s_nop 1\n"
    "v_max_i32_dpp v209, v209, v209 row_ror:1 row_mask:0xf bank_mask:0xf\n"
    "s_nop 1\n"
    "v_max_i32_dpp v209, v209, v209 row_ror:2 row_mask:0xf bank_mask:0xf\n"
    "s_nop 1\n"
    "v_max_i32_dpp v209, v209, v209 row_ror:4 row_mask:0xf bank_mask:0xf\n"
    "s_nop 1\n"
    "v_max_i32_dpp v209, v209, v209 row_ror:8 row_mask:0xf bank_mask:0xf\n"
    "v_mov_b32 v210, v209\n"
    "s_nop 1\n"
    "v_permlane16_swap_b32 v209, v210\n"
    "v_max_i32 v209, v209, v210\n"
    "v_mov_b32 v210, v209\n"
    "s_nop 1\n"
    "v_permlane32_swap_b32 v209, v210\n"
    "v_max_i32 v209, v209, v210\n"
    "s_nop 0\n"
    "v_readfirstlane_b32 s84, v209\n"
    "s_sub_i32 s85, s84, 217\n"
    "s_and_b32 s85, s85, -4\n"
    "s_cmp_gt_i32 s84, 280\n"
    "s_cselect_b32 s85, s85, 0\n"
    "s_add_u32 s11, s11, s85\n"
    "s_lshl_b32 s84, s61, 31\n"
    "v_or_b32 v120, s84, v120\n"
    "v_add_u32 v117, 0x400000, v117\n"
    "v_add_u32 v118, 0x400000, v118\n"
    "s_bfe_u32 s85, s61, 0x20001\n"
    "s_lshl_b32 s84, s85, 10\n"
    "s_add_u32 s84, s84, s68\n"
    "s_add_u32 s87, s84, 0xe8000\n"
    "v_lshl_add_u32 v5, v1, 4, s87\n"
    "s_mov_b32 s66, 0xe8000\n"
    "s_branch Ldj_25\n"
    "Ld2_24:\n"
    "s_lshl_b32 s96, s57, 2\n"
    "s_lshl_b32 s86, s64, 1\n"
    "s_add_u32 s96, s96, s86\n"
    "s_bfe_u32 s85, s59, 0x10000\n"
    "s_add_u32 s96, s96, s85\n"
    "s_mul_i32 s5, s59, 28672\n"
    "s_lshl_b32 s84, s96, 5\n"
    "v_lshl_add_u32 v206, v3, 10, s84\n"
    "v_lshl_add_u32 v206, v2, 1, v206\n"
    "s_mov_b64 exec, 0\n"
    "s_cmp_eq_u32 s59, 0\n"
    "s_cbranch_scc0 Lwn_78\n"
    "s_call_b64 s[0:1], Lseg_D20_0\n"
    "Lwn_78:\n"
    "s_cmp_eq_u32 s59, 1\n"
    "s_cbranch_scc0 Lwn_79\n"
    "s_call_b64 s[0:1], Lseg_A1_0\n"
    "Lwn_79:\n"
    "s_cmp_eq_u32 s59, 2\n"
    "s_cbranch_scc0 Lwn_80\n"
    "s_call_b64 s[0:1], Lseg_B1_0\n"
    "Lwn_80:\n"
    "s_cmp_eq_u32 s59, 3\n"
    "s_cbranch_scc0 Lwn_81\n"
    "s_call_b64 s[0:1], Lseg_D21_0\n"
    "Lwn_81:\n"
    "s_mov_b64 exec, -1\n"
    "s_add_u32 s88, s83, 0x20000\n"
    "s_add_u32 s89, s83, 0x21000\n"
    "s_add_u32 s90, s83, 0x24000\n"
    "s_add_u32 s91, s83, 0x25000\n"
    "s_add_u32 s92, s83, 0x28000\n"
    "s_add_u32 s93, s83, 0x29000\n"
    "s_add_u32 s94, s83, 0x2c000\n"
    "s_add_u32 s95, s83, 0x2d000\n"
    "v_and_b32 v194, 1, v1\n"
    "v_mul_u32_u24 v194, 8190, v194\n"
    "v_sub_u32 v204, v121, v194\n"
    "s_lshl_b32 s84, s96, 7\n"
    "s_add_u32 s84, s84, s68\n"
    "v_add_u32 v204, s84, v204\n"
    "v_and_b32 v194, 1, v9\n"
    "s_mul_i32 s6, s59, 28\n"
    "s_add_u32 s6, s6, 0\n"
    "s_bfe_u32 s85, s6, 0x60000\n"
    "s_lshl_b32 s84, s85, 7\n"
    "s_bfe_u32 s85, s6, 0x10006\n"
    "s_lshl_b32 s86, s85, 14\n"
    "s_add_u32 s84, s84, s86\n"
    "s_add_u32 s84, s84, s68\n"
    "s_add_u32 s87, s84, 0x30000\n"
    "v_lshl_add_u32 v200, v1, 4, s87\n"
    "s_mul_i32 s6, s59, 28\n"
    "s_add_u32 s6, s6, 8\n"
    "s_bfe_u32 s85, s6, 0x60000\n"
    "s_lshl_b32 s84, s85, 7\n"
    "s_bfe_u32 s85, s6, 0x10006\n"
    "s_lshl_b32 s86, s85, 14\n"
    "s_add_u32 s84, s84, s86\n"
    "s_add_u32 s84, s84, s68\n"
    "s_add_u32 s87, s84, 0x30000\n"
    "v_lshl_add_u32 v201, v1, 4, s87\n"
    "s_mul_i32 s6, s59, 28\n"
    "s_add_u32 s6, s6, 16\n"
    "s_bfe_u32 s85, s6, 0x60000\n"
    "s_lshl_b32 s84, s85, 7\n"
    "s_bfe_u32 s85, s6, 0x10006\n"
    "s_lshl_b32 s86, s85, 14\n"
    "s_add_u32 s84, s84, s86\n"
    "s_add_u32 s84, s84, s68\n"
    "s_add_u32 s87, s84, 0x30000\n"
    "v_lshl_add_u32 v202, v1, 4, s87\n"
    "s_mul_i32 s6, s59, 28\n"
    "s_add_u32 s6, s6, 24\n"
    "s_bfe_u32 s85, s6, 0x60000\n"
    "s_lshl_b32 s84, s85, 7\n"
    "s_bfe_u32 s85, s6, 0x10006\n"
    "s_lshl_b32 s86, s85, 14\n"
    "s_add_u32 s84, s84, s86\n"
    "s_add_u32 s84, s84, s68\n"
    "s_add_u32 s87, s84, 0x30000\n"
    "v_lshl_add_u32 v203, v3, 7, s87\n"
    "v_lshl_add_u32 v203, v194, 6, v203\n"
    "v_lshl_add_u32 v203, v10, 4, v203\n"
    "s_mul_i32 s84, s59, 1280\n"
    "s_add_u32 s87, s84, 0x21000\n"
    "v_mul_u32_u24 v230, 288, v3\n"
    "v_add_u32 v6, s87, v230\n"
    "v_lshl_add_u32 v6, v2, 4, v6\n"
    "s_mov_b32 s87, 0x21000\n"
    "v_mul_u32_u24 v230, 288, v9\n"
    "v_add_u32 v205, s87, v230\n"
    "v_lshl_add_u32 v205, v3, 4, v205\n"
    "v_lshl_add_u32 v205, v10, 2, v205\n"
    "s_bfe_u32 s85, s59, 0x10000\n"
    "s_mul_i32 s84, s85, 5120\n"
    "v_add_u32 v205, s84, v205\n"
    "s_lshl_b32 s84, s96, 7\n"
    "s_add_u32 s6, s84, 0xe8000\n"
    "v_lshl_add_u32 v194, v3, 11, s5\n"
    "v_lshl_add_u32 v194, v9, 6, v194\n"
    "v_lshl_add_u32 v194, v10, 3, v194\n"
    "s_add_u32 s87, s5, 0x6000\n"
    "v_lshl_add_u32 v195, v3, 10, s87\n"
    "v_lshl_add_u32 v195, v9, 6, v195\n"
    "v_lshl_add_u32 v195, v10, 3, v195\n"
    "buffer_load_ushort v7, v206, s[20:23], 0 offen\n"
    "v_lshrrev_b32 v220, 2, v1\n"
    "s_mul_i32 s84, s59, 458752\n"
    "s_lshl_b32 s86, s57, 7\n"
    "s_add_u32 s84, s84, s86\n"
    "s_lshl_b32 s86, s64, 6\n"
    "s_add_u32 s84, s84, s86\n"
    "v_lshl_add_u32 v238, v220, 10, s84\n"
    "v_lshl_add_u32 v238, v10, 4, v238\n"
    "s_mov_b32 s87, 0\n"
    "s_add_u32 m0, s5, 0\n"
    "s_nop 0\n"
    "buffer_load_dwordx4 v238, s[48:51], s87 offen offset:0 lds\n"
    "s_mov_b32 s87, 16384\n"
    "s_add_u32 m0, s5, 1024\n"
    "s_nop 0\n"
    "buffer_load_dwordx4 v238, s[48:51], s87 offen offset:0 lds\n"
    "s_mov_b32 s87, 32768\n"
    "s_add_u32 m0, s5, 2048\n"
    "s_nop 0\n"
    "buffer_load_dwordx4 v238, s[48:51], s87 offen offset:0 lds\n"
    "s_mov_b32 s87, 49152\n"
    "s_add_u32 m0, s5, 3072\n"
    "s_nop 0\n"
    "buffer_load_dwordx4 v238, s[48:51], s87 offen offset:0 lds\n"
    "s_mov_b32 s87, 65536\n"
    "s_add_u32 m0, s5, 4096\n"
    "s_nop 0\n"
    "buffer_load_dwordx4 v238, s[48:51], s87 offen offset:0 lds\n"
    "s_mov_b32 s87, 81920\n"
    "s_add_u32 m0, s5, 5120\n"
    "s_nop 0\n"
    "buffer_load_dwordx4 v238, s[48:51], s87 offen offset:0 lds\n"
    "s_mov_b32 s87, 98304\n"
    "s_add_u32 m0, s5, 6144\n"
    "s_nop 0\n"
    "buffer_load_dwordx4 v238, s[48:51], s87 offen offset:0 lds\n"
    "s_mov_b32 s87, 114688\n"
    "s_add_u32 m0, s5, 7168\n"
    "s_nop 0\n"
    "buffer_load_dwordx4 v238, s[48:51], s87 offen offset:0 lds\n"
    "s_mov_b32 s87, 131072\n"
    "s_add_u32 m0, s5, 8192\n"
    "s_nop 0\n"
    "buffer_load_dwordx4 v238, s[48:51], s87 offen offset:0 lds\n"
    "s_mov_b32 s87, 147456\n"
    "s_add_u32 m0, s5, 9216\n"
    "s_nop 0\n"
    "buffer_load_dwordx4 v238, s[48:51], s87 offen offset:0 lds\n"
    "s_mov_b32 s87, 163840\n"
    "s_add_u32 m0, s5, 10240\n"
    "s_nop 0\n"
    "buffer_load_dwordx4 v238, s[48:51], s87 offen offset:0 lds\n"
    "s_mov_b32 s87, 180224\n"
    "s_add_u32 m0, s5, 11264\n"
    "s_nop 0\n"
    "buffer_load_dwordx4 v238, s[48:51], s87 offen offset:0 lds\n"
    "s_mov_b32 s87, 196608\n"
    "s_add_u32 m0, s5, 12288\n"
    "s_nop 0\n"
    "buffer_load_dwordx4 v238, s[48:51], s87 offen offset:0 lds\n"
    "s_mov_b32 s87, 212992\n"
    "s_add_u32 m0, s5, 13312\n"
    "s_nop 0\n"
    "buffer_load_dwordx4 v238, s[48:51], s87 offen offset:0 lds\n"
    "s_mov_b32 s87, 229376\n"
    "s_add_u32 m0, s5, 14336\n"
    "s_nop 0\n"
    "buffer_load_dwordx4 v238, s[48:51], s87 offen offset:0 lds\n"
    "s_mov_b32 s87, 245760\n"
    "s_add_u32 m0, s5, 15360\n"
    "s_nop 0\n"
    "buffer_load_dwordx4 v238, s[48:51], s87 offen offset:0 lds\n"
    "s_mov_b32 s87, 262144\n"
    "s_add_u32 m0, s5, 16384\n"
    "s_nop 0\n"
    "buffer_load_dwordx4 v238, s[48:51], s87 offen offset:0 lds\n"
    "s_mov_b32 s87, 278528\n"
    "s_add_u32 m0, s5, 17408\n"
    "s_nop 0\n"
    "buffer_load_dwordx4 v238, s[48:51], s87 offen offset:0 lds\n"
    "s_mov_b32 s87, 294912\n"
    "s_add_u32 m0, s5, 18432\n"
    "s_nop 0\n"
    "buffer_load_dwordx4 v238, s[48:51], s87 offen offset:0 lds\n"
    "s_mov_b32 s87, 311296\n"
    "s_add_u32 m0, s5, 19456\n"
    "s_nop 0\n"
    "buffer_load_dwordx4 v238, s[48:51], s87 offen offset:0 lds\n"
    "s_mov_b32 s87, 327680\n"
    "s_add_u32 m0, s5, 20480\n"
    "s_nop 0\n"
    "buffer_load_dwordx4 v238, s[48:51], s87 offen offset:0 lds\n"
    "s_mov_b32 s87, 344064\n"
    "s_add_u32 m0, s5, 21504\n"
    "s_nop 0\n"
    "buffer_load_dwordx4 v238, s[48:51], s87 offen offset:0 lds\n"
    "s_mov_b32 s87, 360448\n"
    "s_add_u32 m0, s5, 22528\n"
    "s_nop 0\n"
    "buffer_load_dwordx4 v238, s[48:51], s87 offen offset:0 lds\n"
    "s_mov_b32 s87, 376832\n"
    "s_add_u32 m0, s5, 23552\n"
    "s_nop 0\n"
    "buffer_load_dwordx4 v238, s[48:51], s87 offen offset:0 lds\n"
    "s_mov_b32 s87, 393216\n"
    "s_add_u32 m0, s5, 24576\n"
    "s_nop 0\n"
    "buffer_load_dwordx4 v238, s[48:51], s87 offen offset:0 lds\n"
    "s_mov_b32 s87, 409600\n"
    "s_add_u32 m0, s5, 25600\n"
    "s_nop 0\n"
    "buffer_load_dwordx4 v238, s[48:51], s87 offen offset:0 lds\n"
    "s_mov_b32 s87, 425984\n"
    "s_add_u32 m0, s5, 26624\n"
    "s_nop 0\n"
    "buffer_load_dwordx4 v238, s[48:51], s87 offen offset:0 lds\n"
    "s_mov_b32 s87, 442368\n"
    "s_add_u32 m0, s5, 27648\n"
    "s_nop 0\n"
    "buffer_load_dwordx4 v238, s[48:51], s87 offen offset:0 lds\n"
    "buffer_load_dwordx4 v[232:235], v236, s[12:15], 0 offen sc1\n"
    "Lbcc_82:\n"
    "s_waitcnt vmcnt(0)\n"
    "v_max3_u32 v230, v232, v233, v234\n"
    "v_max_u32 v230, v230, v235\n"
    "v_cmp_eq_u32 vcc, -1, v230\n"
    "s_cbranch_vccnz Lbcs_83\n"
    "Lbcd_84:\n"
    "v_mov_b32 v168, 0x3f803f80\n"
    "v_mov_b32 v169, 0x3f803f80\n"
    "v_mov_b32 v170, 0x3f803f80\n"
    "v_mov_b32 v171, 0x3f803f80\n"
    "s_nop 1\n"
    "v_mfma_f32_16x16x32_bf16 v[172:175], v[16:19], v[168:171], 0\n"
    "v_mfma_f32_16x16x32_bf16 v[172:175], v[20:23], v[168:171], v[172:175]\n"
    "v_mfma_f32_16x16x32_bf16 v[172:175], v[24:27], v[168:171], v[172:175]\n"
    "v_mfma_f32_16x16x32_bf16 v[172:175], v[28:31], v[168:171], v[172:175]\n"
    "v_subrev_u32 v232, s11, v232\n"
    "v_subrev_u32 v233, s11, v233\n"
    "v_subrev_u32 v234, s11, v234\n"
    "v_subrev_u32 v235, s11, v235\n"
    "v_max3_i32 v226, v232, v233, v234\n"
    "v_max_i32 v226, v226, v235\n"
    "s_nop 1\n"
    "v_max_i32_dpp v226, v226, v226 row_ror:1 row_mask:0xf bank_mask:0xf\n"
    "s_nop 1\n"
    "v_max_i32_dpp v226, v226, v226 row_ror:2 row_mask:0xf bank_mask:0xf\n"
    "s_nop 1\n"
    "v_max_i32_dpp v226, v226, v226 row_ror:4 row_mask:0xf bank_mask:0xf\n"
    "s_nop 1\n"
    "v_max_i32_dpp v226, v226, v226 row_ror:8 row_mask:0xf bank_mask:0xf\n"
    "v_mov_b32 v227, v226\n"
    "s_nop 1\n"
    "v_permlane16_swap_b32 v226, v227\n"
    "v_max_i32 v226, v226, v227\n"
    "v_mov_b32 v227, v226\n"
    "s_nop 1\n"
    "v_permlane32_swap_b32 v226, v227\n"
    "v_max_i32 v226, v226, v227\n"
    "s_nop 0\n"
    "v_readfirstlane_b32 s84, v226\n"
    "s_add_u32 s11, s11, s84\n"
    "v_cvt_pk_bf16_f32 v180, v172, v173\n"
    "v_cvt_pk_bf16_f32 v181, v174, v175\n"
    "s_mov_b32 s84, 0x50000\n"
    "s_nop 0\n"
    "buffer_store_dwordx2 v[180:181], v112, s[12:15], s84 offen sc1\n"
    "v_add_u32 v4, s84, v113\n"
    "Lseg_D20_0:\n"
    "s_cbranch_execz Lseg_D20_1\n"
    "v_mov_b32 v144, -1\n"
    "v_mov_b32 v145, -1\n"
    "v_mov_b32 v146, -1\n"
    "v_mov_b32 v147, -1\n"
    "s_mul_i32 s84, s57, 73728\n"
    "s_mul_i32 s86, s97, 4608\n"
    "s_add_u32 s84, s84, s86\n"
    "v_lshl_add_u32 v148, v0, 4, s84\n"
    "buffer_store_dwordx4 v[144:147], v148, s[12:15], 0 offen sc1\n"
    "buffer_store_dwordx4 v[144:147], v148, s[12:15], 0 offen offset:512 sc1\n"
    "v_lshlrev_b32 v7, 16, v7\n"
    "s_lshl_b32 s84, s97, 7\n"
    "s_lshl_b32 s86, s57, 2\n"
    "s_add_u32 s84, s84, s86\n"
    "s_add_u32 s87, s84, 0x144000\n"
    "v_lshl_add_u32 v160, v0, 13, s87\n"
    "v_mov_b32 v161, 0x80000000\n"
    "v_cmp_gt_u32 vcc, 2, v0\n"
    "s_nop 1\n"
    "v_cndmask_b32 v160, v161, v160, vcc\n"
    "v_bfe_u32 v157, v1, 1, 4\n"
    "v_and_b32 v158, 1, v1\n"
    "s_add_u32 s87, s68, 0x148000\n"
    "v_lshl_add_u32 v156, v157, 7, s87\n"
    "v_lshl_add_u32 v156, v158, 4, v156\n"
    "Lwt_85:\n"
    "s_memrealtime s[78:79]\n"
    "s_waitcnt lgkmcnt(0)\n"
    "s_sub_u32 s78, s78, s11\n"
    "s_cmp_lt_u32 s78, 192\n"
    "s_cbranch_scc1 Lwt_85\n"
    "buffer_load_dwordx4 v[152:155], v156, s[12:15], 0 offen sc1\n"
    "ds_read_b64_tr_b16 v[48:49], v194 offset:0\n"
    "ds_read_b64_tr_b16 v[50:51], v194 offset:256\n"
    "ds_read_b64_tr_b16 v[52:53], v194 offset:32\n"
    "ds_read_b64_tr_b16 v[54:55], v194 offset:288\n"
    "ds_read_b64_tr_b16 v[56:57], v194 offset:512\n"
    "ds_read_b64_tr_b16 v[58:59], v194 offset:768\n"
    "ds_read_b64_tr_b16 v[60:61], v194 offset:544\n"
    "ds_read_b64_tr_b16 v[62:63], v194 offset:800\n"
    "ds_read_b64_tr_b16 v[64:65], v194 offset:1024\n"
    "ds_read_b64_tr_b16 v[66:67], v194 offset:1280\n"
    "ds_read_b64_tr_b16 v[68:69], v194 offset:1056\n"
    "ds_read_b64_tr_b16 v[70:71], v194 offset:1312\n"
    "ds_read_b64_tr_b16 v[72:73], v194 offset:1536\n"
    "ds_read_b64_tr_b16 v[74:75], v194 offset:1792\n"
    "ds_read_b64_tr_b16 v[76:77], v194 offset:1568\n"
    "ds_read_b64_tr_b16 v[78:79], v194 offset:1824\n"
    "ds_read_b64_tr_b16 v[80:81], v194 offset:8192\n"
    "ds_read_b64_tr_b16 v[82:83], v194 offset:8448\n"
    "ds_read_b64_tr_b16 v[84:85], v194 offset:8224\n"
    "ds_read_b64_tr_b16 v[86:87], v194 offset:8480\n"
    "ds_read_b64_tr_b16 v[88:89], v194 offset:8704\n"
    "ds_read_b64_tr_b16 v[90:91], v194 offset:8960\n"
    "ds_read_b64_tr_b16 v[92:93], v194 offset:8736\n"
    "ds_read_b64_tr_b16 v[94:95], v194 offset:8992\n"
    "ds_read_b64_tr_b16 v[96:97], v194 offset:9216\n"
    "ds_read_b64_tr_b16 v[98:99], v194 offset:9472\n"
    "ds_read_b64_tr_b16 v[100:101], v194 offset:9248\n"
    "ds_read_b64_tr_b16 v[102:103], v194 offset:9504\n"
    "ds_read_b64_tr_b16 v[104:105], v194 offset:9728\n"
    "ds_read_b64_tr_b16 v[106:107], v194 offset:9984\n"
    "ds_read_b64_tr_b16 v[108:109], v194 offset:9760\n"
    "ds_read_b64_tr_b16 v[110:111], v194 offset:10016\n"
    "ds_read_b64_tr_b16 v[208:209], v194 offset:16384\n"
    "ds_read_b64_tr_b16 v[210:211], v194 offset:16640\n"
    "ds_read_b64_tr_b16 v[212:213], v194 offset:16416\n"
    "ds_read_b64_tr_b16 v[214:215], v194 offset:16672\n"
    "ds_read_b64_tr_b16 v[216:217], v194 offset:16896\n"
    "ds_read_b64_tr_b16 v[218:219], v194 offset:17152\n"
    "ds_read_b64_tr_b16 v[220:221], v194 offset:16928\n"
    "ds_read_b64_tr_b16 v[222:223], v194 offset:17184\n"
    "ds_read_b64_tr_b16 v[224:225], v194 offset:17408\n"
    "ds_read_b64_tr_b16 v[226:227], v194 offset:17664\n"
    "ds_read_b64_tr_b16 v[228:229], v194 offset:17440\n"
    "ds_read_b64_tr_b16 v[230:231], v194 offset:17696\n"
    "ds_read_b64_tr_b16 v[232:233], v194 offset:17920\n"
    "ds_read_b64_tr_b16 v[234:235], v194 offset:18176\n"
    "ds_read_b64_tr_b16 v[236:237], v194 offset:17952\n"
    "ds_read_b64_tr_b16 v[238:239], v194 offset:18208\n"
    "ds_read_b64_tr_b16 v[240:241], v195 offset:0\n"
    "ds_read_b64_tr_b16 v[242:243], v195 offset:256\n"
    "ds_read_b64_tr_b16 v[244:245], v195 offset:32\n"
    "ds_read_b64_tr_b16 v[246:247], v195 offset:288\n"
    "Lseg_D20_1:\n"
    "s_cbranch_execz Lseg_D20_2\n"
    "ds_read_b64_tr_b16 v[248:249], v195 offset:512\n"
    "ds_read_b64_tr_b16 v[250:251], v195 offset:768\n"
    "ds_read_b64_tr_b16 v[252:253], v195 offset:544\n"
    "ds_read_b64_tr_b16 v[254:255], v195 offset:800\n"
    "Lac0c_86:\n"
    "s_waitcnt vmcnt(0)\n"
    "v_max3_u32 v157, v152, v153, v154\n"
    "v_max_u32 v157, v157, v155\n"
    "v_cmp_eq_u32 vcc, -1, v157\n"
    "s_cbranch_vccnz Lac0s_87\n"
    "Lac0d_88:\n"
    "v_subrev_u32 v152, s11, v152\n"
    "v_subrev_u32 v153, s11, v153\n"
    "v_subrev_u32 v154, s11, v154\n"
    "v_subrev_u32 v155, s11, v155\n"
    "v_max3_i32 v157, v152, v153, v154\n"
    "v_max_i32 v157, v157, v155\n"
    "s_nop 1\n"
    "v_max_i32_dpp v157, v157, v157 row_ror:1 row_mask:0xf bank_mask:0xf\n"
    "s_nop 1\n"
    "v_max_i32_dpp v157, v157, v157 row_ror:2 row_mask:0xf bank_mask:0xf\n"
    "s_nop 1\n"
    "v_max_i32_dpp v157, v157, v157 row_ror:4 row_mask:0xf bank_mask:0xf\n"
    "s_nop 1\n"
    "v_max_i32_dpp v157, v157, v157 row_ror:8 row_mask:0xf bank_mask:0xf\n"
    "v_mov_b32 v158, v157\n"
    "s_nop 1\n"
    "v_permlane16_swap_b32 v157, v158\n"
    "v_max_i32 v157, v157, v158\n"
    "v_mov_b32 v158, v157\n"
    "s_nop 1\n"
    "v_permlane32_swap_b32 v157, v158\n"
    "v_max_i32 v157, v157, v158\n"
    "s_nop 0\n"
    "v_readfirstlane_b32 s84, v157\n"
    "s_sub_i32 s85, s84, 121\n"
    "s_and_b32 s85, s85, -4\n"
    "s_cmp_gt_i32 s84, 172\n"
    "s_cselect_b32 s85, s85, 0\n"
    "s_add_u32 s11, s11, s85\n"
    "s_memrealtime s[80:81]\n"
    "s_waitcnt lgkmcnt(0)\n"
    "s_sub_u32 s80, s80, 40\n"
    "s_min_u32 s80, s80, 0xfffffffe\n"
    "v_mov_b32 v161, s80\n"
    "buffer_store_dword v161, v160, s[12:15], 0 offen sc1\n"
    "Lwt_89:\n"
    "s_memrealtime s[78:79]\n"
    "s_waitcnt lgkmcnt(0)\n"
    "s_sub_u32 s78, s78, s11\n"
    "s_cmp_lt_u32 s78, 280\n"
    "s_cbranch_scc1 Lwt_89\n"
    "buffer_load_ushort v144, v204, s[12:15], s88 offen sc1\n"
    "buffer_load_ushort v145, v204, s[12:15], s89 offen sc1\n"
    "buffer_load_ushort v146, v204, s[12:15], s90 offen sc1\n"
    "buffer_load_ushort v147, v204, s[12:15], s91 offen sc1\n"
    "buffer_load_ushort v148, v204, s[12:15], s92 offen sc1\n"
    "buffer_load_ushort v149, v204, s[12:15], s93 offen sc1\n"
    "buffer_load_ushort v150, v204, s[12:15], s94 offen sc1\n"
    "buffer_load_ushort v151, v204, s[12:15], s95 offen sc1\n"
    "buffer_load_dwordx4 v[164:167], v4, s[12:15], 0 offen sc1\n"
    "Lhr0c_90:\n"
    "v_mov_b32 v207, v7\n"
    "s_waitcnt vmcnt(8)\n"
    "v_lshlrev_b32 v193, 16, v144\n"
    "v_add_f32 v207, v207, v193\n"
    "s_waitcnt vmcnt(7)\n"
    "v_lshlrev_b32 v193, 16, v145\n"
    "v_add_f32 v207, v207, v193\n"
    "s_waitcnt vmcnt(6)\n"
    "v_lshlrev_b32 v193, 16, v146\n"
    "v_add_f32 v207, v207, v193\n"
    "s_waitcnt vmcnt(5)\n"
    "v_lshlrev_b32 v193, 16, v147\n"
    "v_add_f32 v207, v207, v193\n"
    "s_waitcnt vmcnt(4)\n"
    "v_lshlrev_b32 v193, 16, v148\n"
    "v_add_f32 v207, v207, v193\n"
    "s_waitcnt vmcnt(3)\n"
    "v_lshlrev_b32 v193, 16, v149\n"
    "v_add_f32 v207, v207, v193\n"
    "s_waitcnt vmcnt(2)\n"
    "v_lshlrev_b32 v193, 16, v150\n"
    "v_add_f32 v207, v207, v193\n"
    "s_waitcnt vmcnt(1)\n"
    "v_lshlrev_b32 v193, 16, v151\n"
    "v_add_f32 v207, v207, v193\n"
    "v_cmp_u_f32 vcc, v207, v207\n"
    "s_cbranch_vccnz Lhr0s_91\n"
    "Lhr0d_92:\n"
    "Lqcc_93:\n"
    "s_waitcnt vmcnt(0)\n"
    "v_mfma_f32_16x16x32_bf16 v[168:171], v[164:167], v[32:35], 0\n"
    "v_mfma_f32_16x16x32_bf16 v[172:175], v[164:167], v[36:39], 0\n"
    "v_mfma_f32_16x16x32_bf16 v[176:179], v[164:167], v[40:43], 0\n"
    "v_mfma_f32_16x16x32_bf16 v[180:183], v[164:167], v[44:47], 0\n"
    "s_nop 6\n"
    "v_permlane32_swap_b32 v168, v176\n"
    "v_permlane32_swap_b32 v172, v180\n"
    "v_add_f32 v168, v168, v176\n"
    "v_add_f32 v172, v172, v180\n"
    "s_nop 1\n"
    "v_permlane16_swap_b32 v168, v172\n"
    "v_add_f32 v168, v168, v172\n"
    "ds_write_b32 v139, v168 offset:7168\n"
    "v_cmp_u_f32 vcc, v168, v168\n"
    "s_cbranch_vccnz Lqcs_94\n"
    "Lqcd_95:\n"
    "s_waitcnt lgkmcnt(0)\n"
    "s_barrier\n"
    "ds_read2st64_b32 v[164:165], v140 offset0:28 offset1:32\n"
    "ds_read2st64_b32 v[166:167], v140 offset0:36 offset1:40\n"
    "s_waitcnt lgkmcnt(0)\n"
    "v_pk_add_f32 v[164:165], v[164:165], v[166:167]\n"
    "v_add_f32 v4, v164, v165\n"
    "s_barrier\n"
    "s_lshl_b32 s84, s61, 31\n"
    "v_or_b32 v120, s84, v120\n"
    "v_add_u32 v117, 0x400000, v117\n"
    "v_add_u32 v118, 0x400000, v118\n"
    "s_bfe_u32 s85, s61, 0x20001\n"
    "s_lshl_b32 s84, s85, 10\n"
    "s_add_u32 s84, s84, s68\n"
    "s_add_u32 s87, s84, 0xe8000\n"
    "v_lshl_add_u32 v5, v1, 4, s87\n"
    "s_mov_b32 s66, 0xe8000\n"
    "v_bfe_u32 v149, v1, 1, 4\n"
    "v_and_b32 v150, 1, v1\n"
    "s_add_u32 s87, s68, 0x144000\n"
    "v_lshl_add_u32 v148, v149, 7, s87\n"
    "v_lshl_add_u32 v148, v150, 4, v148\n"
    "Lwt_96:\n"
    "s_memrealtime s[78:79]\n"
    "s_waitcnt lgkmcnt(0)\n"
    "s_sub_u32 s78, s78, s11\n"
    "s_cmp_lt_u32 s78, 376\n"
    "s_cbranch_scc1 Lwt_96\n"
    "buffer_load_dwordx4 v[152:155], v200, s[12:15], s83 offen sc1\n"
    "s_nop 11\n"
    "buffer_load_dwordx4 v[156:159], v201, s[12:15], s83 offen sc1\n"
    "s_nop 11\n"
    "buffer_load_dwordx4 v[160:163], v202, s[12:15], s83 offen sc1\n"
    "s_nop 11\n"
    "buffer_load_dwordx4 v[164:167], v203, s[12:15], s83 offen sc1\n"
    "Lh50c_97:\n"
    "buffer_load_dwordx4 v[144:147], v148, s[12:15], 0 offen sc1\n"
    "s_waitcnt vmcnt(4)\n"
    "v_mfma_f32_16x16x32_bf16 v[168:171], v[48:51], v[152:155], 0\n"
    "v_mfma_f32_16x16x32_bf16 v[172:175], v[56:59], v[152:155], 0\n"
    "v_mfma_f32_16x16x32_bf16 v[176:179], v[64:67], v[152:155], 0\n"
    "v_mfma_f32_16x16x32_bf16 v[180:183], v[72:75], v[152:155], 0\n"
    "v_mfma_f32_16x16x32_bf16 v[184:187], v[52:55], v[152:155], 0\n"
    "v_mfma_f32_16x16x32_bf16 v[188:191], v[60:63], v[152:155], 0\n"
    "v_mfma_f32_16x16x32_bf16 v[192:195], v[68:71], v[152:155], 0\n"
    "v_mfma_f32_16x16x32_bf16 v[196:199], v[76:79], v[152:155], 0\n"
    "s_waitcnt vmcnt(3)\n"
    "v_mfma_f32_16x16x32_bf16 v[168:171], v[80:83], v[156:159], v[168:171]\n"
    "v_mfma_f32_16x16x32_bf16 v[172:175], v[88:91], v[156:159], v[172:175]\n"
    "v_mfma_f32_16x16x32_bf16 v[176:179], v[96:99], v[156:159], v[176:179]\n"
    "v_mfma_f32_16x16x32_bf16 v[180:183], v[104:107], v[156:159], v[180:183]\n"
    "v_mfma_f32_16x16x32_bf16 v[184:187], v[84:87], v[156:159], v[184:187]\n"
    "v_mfma_f32_16x16x32_bf16 v[188:191], v[92:95], v[156:159], v[188:191]\n"
    "v_mfma_f32_16x16x32_bf16 v[192:195], v[100:103], v[156:159], v[192:195]\n"
    "v_mfma_f32_16x16x32_bf16 v[196:199], v[108:111], v[156:159], v[196:199]\n"
    "s_waitcnt vmcnt(2)\n"
    "v_mfma_f32_16x16x32_bf16 v[176:179], v[224:227], v[160:163], v[176:179]\n"
    "v_mfma_f32_16x16x32_bf16 v[180:183], v[232:235], v[160:163], v[180:183]\n"
    "v_mfma_f32_16x16x32_bf16 v[168:171], v[208:211], v[160:163], v[168:171]\n"
    "v_mfma_f32_16x16x32_bf16 v[172:175], v[216:219], v[160:163], v[172:175]\n"
    "s_waitcnt vmcnt(1)\n"
    "v_mfma_f32_16x16x32_bf16 v[168:171], v[240:243], v[164:167], v[168:171]\n"
    "v_mfma_f32_16x16x32_bf16 v[172:175], v[248:251], v[164:167], v[172:175]\n"
    "v_mfma_f32_16x16x32_bf16 v[192:195], v[228:231], v[160:163], v[192:195]\n"
    "s_nop 1\n"
    "v_add_f32_dpp v176, v180, v176 row_shl:4 row_mask:0xf bank_mask:0xf bound_ctrl:1\n"
    "v_add_f32_dpp v177, v181, v177 row_shl:4 row_mask:0xf bank_mask:0xf bound_ctrl:1\n"
    "v_mfma_f32_16x16x32_bf16 v[196:199], v[236:239], v[160:163], v[196:199]\n"
    "v_add_f32_dpp v178, v182, v178 row_shl:4 row_mask:0xf bank_mask:0xf bound_ctrl:1\n"
    "v_add_f32_dpp v179, v183, v179 row_shl:4 row_mask:0xf bank_mask:0xf bound_ctrl:1\n"
    "v_mfma_f32_16x16x32_bf16 v[184:187], v[212:215], v[160:163], v[184:187]\n"
    "v_mfma_f32_16x16x32_bf16 v[188:191], v[220:223], v[160:163], v[188:191]\n"
    "v_cmp_u_f32 vcc, v168, v168\n"
    "v_add_f32_dpp v168, v176, v168 row_shl:8 row_mask:0xf bank_mask:0xf bound_ctrl:1\n"
    "Lseg_D20_2:\n"
    "s_cbranch_execz Ltret\n"
    "v_mfma_f32_16x16x32_bf16 v[184:187], v[244:247], v[164:167], v[184:187]\n"
    "v_add_f32_dpp v169, v177, v169 row_shl:8 row_mask:0xf bank_mask:0xf bound_ctrl:1\n"
    "v_add_f32_dpp v170, v178, v170 row_shl:8 row_mask:0xf bank_mask:0xf bound_ctrl:1\n"
    "v_mfma_f32_16x16x32_bf16 v[188:191], v[252:255], v[164:167], v[188:191]\n"
    "v_add_f32_dpp v171, v179, v171 row_shl:8 row_mask:0xf bank_mask:0xf bound_ctrl:1\n"
    "v_add_f32_dpp v168, v172, v168 row_shl:4 row_mask:0xf bank_mask:0xf bound_ctrl:1\n"
    "v_add_f32_dpp v169, v173, v169 row_shl:4 row_mask:0xf bank_mask:0xf bound_ctrl:1\n"
    "v_add_f32_dpp v170, v174, v170 row_shl:4 row_mask:0xf bank_mask:0xf bound_ctrl:1\n"
    "v_add_f32_dpp v171, v175, v171 row_shl:4 row_mask:0xf bank_mask:0xf bound_ctrl:1\n"
    "ds_write_b128 v6, v[168:171]\n"
    "v_add_f32_dpp v192, v196, v192 row_shl:4 row_mask:0xf bank_mask:0xf bound_ctrl:1\n"
    "v_add_f32_dpp v193, v197, v193 row_shl:4 row_mask:0xf bank_mask:0xf bound_ctrl:1\n"
    "v_add_f32_dpp v194, v198, v194 row_shl:4 row_mask:0xf bank_mask:0xf bound_ctrl:1\n"
    "v_add_f32_dpp v195, v199, v195 row_shl:4 row_mask:0xf bank_mask:0xf bound_ctrl:1\n"
    "v_add_f32_dpp v184, v192, v184 row_shl:8 row_mask:0xf bank_mask:0xf bound_ctrl:1\n"
    "v_add_f32_dpp v185, v193, v185 row_shl:8 row_mask:0xf bank_mask:0xf bound_ctrl:1\n"
    "v_add_f32_dpp v186, v194, v186 row_shl:8 row_mask:0xf bank_mask:0xf bound_ctrl:1\n"
    "v_add_f32_dpp v187, v195, v187 row_shl:8 row_mask:0xf bank_mask:0xf bound_ctrl:1\n"
    "v_add_f32_dpp v184, v188, v184 row_shl:4 row_mask:0xf bank_mask:0xf bound_ctrl:1\n"
    "v_add_f32_dpp v185, v189, v185 row_shl:4 row_mask:0xf bank_mask:0xf bound_ctrl:1\n"
    "v_add_f32_dpp v186, v190, v186 row_shl:4 row_mask:0xf bank_mask:0xf bound_ctrl:1\n"
    "v_add_f32_dpp v187, v191, v187 row_shl:4 row_mask:0xf bank_mask:0xf bound_ctrl:1\n"
    "ds_write_b128 v6, v[184:187] offset:5120\n"
    "s_cbranch_vccnz Lh50s_98\n"
    "Lh50d_99:\n"
    "s_waitcnt vmcnt(0)\n"
    "s_cmp_lt_u32 s59, 2\n"
    "s_waitcnt lgkmcnt(0)\n"
    "s_barrier\n"
    "s_cbranch_scc0 Lsk_103\n"
    "ds_read2st64_b32 v[152:153], v205 offset0:0 offset1:5\n"
    "ds_read2st64_b32 v[154:155], v205 offset0:10 offset1:15\n"
    "s_waitcnt lgkmcnt(1)\n"
    "v_add_f32 v152, v152, v153\n"
    "v_add_f32 v152, v152, v207\n"
    "s_waitcnt lgkmcnt(0)\n"
    "v_add_f32 v154, v154, v155\n"
    "v_add_f32 v152, v152, v154\n"
    "s_nop 1\n"
    "v_mov_b32_dpp v153, v152 quad_perm:[0,0,2,2] row_mask:0xf bank_mask:0xf\n"
    "v_mov_b32_dpp v154, v152 quad_perm:[1,1,3,3] row_mask:0xf bank_mask:0xf\n"
    "v_cvt_pk_bf16_f32 v153, v153, v154\n"
    "buffer_store_dword v153, v121, s[12:15], s6 offen sc1\n"
    "v_cvt_pk_bf16_f32 v7, v152, v152\n"
    "v_lshlrev_b32 v7, 16, v7\n"
    "Lsk_103:\n"
    "s_nop 0\n"
    "Ladc_100:\n"
    "v_max3_u32 v149, v144, v145, v146\n"
    "v_max_u32 v149, v149, v147\n"
    "v_cmp_eq_u32 vcc, -1, v149\n"
    "s_cbranch_vccnz Lads_101\n"
    "Ladd_102:\n"
    "v_subrev_u32 v144, s11, v144\n"
    "v_subrev_u32 v145, s11, v145\n"
    "v_subrev_u32 v146, s11, v146\n"
    "v_subrev_u32 v147, s11, v147\n"
    "v_max3_i32 v149, v144, v145, v146\n"
    "v_max_i32 v149, v149, v147\n"
    "s_nop 1\n"
    "v_max_i32_dpp v149, v149, v149 row_ror:1 row_mask:0xf bank_mask:0xf\n"
    "s_nop 1\n"
    "v_max_i32_dpp v149, v149, v149 row_ror:2 row_mask:0xf bank_mask:0xf\n"
    "s_nop 1\n"
    "v_max_i32_dpp v149, v149, v149 row_ror:4 row_mask:0xf bank_mask:0xf\n"
    "s_nop 1\n"
    "v_max_i32_dpp v149, v149, v149 row_ror:8 row_mask:0xf bank_mask:0xf\n"
    "v_mov_b32 v150, v149\n"
    "s_nop 1\n"
    "v_permlane16_swap_b32 v149, v150\n"
    "v_max_i32 v149, v149, v150\n"
    "v_mov_b32 v150, v149\n"
    "s_nop 1\n"
    "v_permlane32_swap_b32 v149, v150\n"
    "v_max_i32 v149, v149, v150\n"
    "s_nop 0\n"
    "v_readfirstlane_b32 s84, v149\n"
    "s_sub_i32 s85, s84, 217\n"
    "s_and_b32 s85, s85, -4\n"
    "s_cmp_gt_i32 s84, 280\n"
    "s_cselect_b32 s85, s85, 0\n"
    "s_add_u32 s11, s11, s85\n"
    "Ldj_25:\n"
    "s_nop 1\n"
    "Lseg_A1_0:\n"
    "s_cbranch_execz Ltret\n"
    "s_mov_b32 s83, 0\n"
    "Lwt_104:\n"
    "s_memrealtime s[78:79]\n"
    "s_waitcnt lgkmcnt(0)\n"
    "s_sub_u32 s78, s78, s11\n"
    "s_cmp_lt_u32 s78, 464\n"
    "s_cbranch_scc1 Lwt_104\n"
    "buffer_load_dwordx4 v[160:163], v5, s[12:15], 0 offen sc1\n"
    "s_sleep 1\n"
    "buffer_load_dwordx4 v[12:15], v122, s[12:15], s66 offen sc1\n"
    "v_mov_b32 v146, 0x3f803f80\n"
    "Lhxc_105:\n"
    "s_waitcnt vmcnt(1)\n"
    "v_mov_b32_dpp v164, v160 row_ror:12 row_mask:0xf bank_mask:0xf\n"
    "v_mov_b32_dpp v165, v161 row_ror:12 row_mask:0xf bank_mask:0xf\n"
    "v_mov_b32_dpp v166, v162 row_ror:12 row_mask:0xf bank_mask:0xf\n"
    "v_mov_b32_dpp v167, v163 row_ror:12 row_mask:0xf bank_mask:0xf\n"
    "v_mfma_f32_16x16x32_bf16 v[208:211], v[16:19], v[160:163], 0\n"
    "v_mov_b32_dpp v168, v160 row_ror:8 row_mask:0xf bank_mask:0xf\n"
    "v_mov_b32_dpp v169, v161 row_ror:8 row_mask:0xf bank_mask:0xf\n"
    "v_mov_b32_dpp v170, v162 row_ror:8 row_mask:0xf bank_mask:0xf\n"
    "v_mov_b32_dpp v171, v163 row_ror:8 row_mask:0xf bank_mask:0xf\n"
    "v_mfma_f32_16x16x32_bf16 v[208:211], v[20:23], v[164:167], v[208:211]\n"
    "v_mov_b32_dpp v172, v160 row_ror:4 row_mask:0xf bank_mask:0xf\n"
    "v_mov_b32_dpp v173, v161 row_ror:4 row_mask:0xf bank_mask:0xf\n"
    "v_mov_b32_dpp v174, v162 row_ror:4 row_mask:0xf bank_mask:0xf\n"
    "v_mov_b32_dpp v175, v163 row_ror:4 row_mask:0xf bank_mask:0xf\n"
    "v_mfma_f32_16x16x32_bf16 v[208:211], v[24:27], v[168:171], v[208:211]\n"
    "s_nop 0\n"
    "v_mfma_f32_16x16x32_bf16 v[208:211], v[28:31], v[172:175], v[208:211]\n"
    "s_nop 7\n"
    "v_cmp_u_f32 vcc, v208, v208\n"
    "v_cvt_pk_bf16_f32 v216, v208, v209\n"
    "v_cvt_pk_bf16_f32 v217, v210, v211\n"
    "s_cbranch_vccnz Lhxs_106\n"
    "Lhxd_107:\n"
    "buffer_store_dwordx2 v[216:217], v112, s[12:15], 0 offen sc1\n"
    "Lhbc_108:\n"
    "s_waitcnt vmcnt(1)\n"
    "v_dot2_f32_bf16 v152, v12, v12, 0\n"
    "v_dot2_f32_bf16 v156, v12, v146, 0\n"
    "v_dot2_f32_bf16 v153, v13, v13, 0\n"
    "v_dot2_f32_bf16 v157, v13, v146, 0\n"
    "v_dot2_f32_bf16 v154, v14, v14, 0\n"
    "v_dot2_f32_bf16 v158, v14, v146, 0\n"
    "v_dot2_f32_bf16 v155, v15, v15, 0\n"
    "v_dot2_f32_bf16 v159, v15, v146, 0\n"
    "s_nop 1\n"
    "v_pk_add_f32 v[152:153], v[152:153], v[154:155]\n"
    "v_pk_add_f32 v[156:157], v[156:157], v[158:159]\n"
    "v_add_f32 v148, v152, v153\n"
    "v_add_f32 v149, v156, v157\n"
    "v_cmp_u_f32 vcc, v148, v149\n"
    "s_cbranch_vccnz Lhbs_109\n"
    "Lhbd_110:\n"
    "v_permlane32_swap_b32 v148, v149\n"
    "v_add_f32 v150, v148, v149\n"
    "v_add_f32 v151, v148, v149\n"
    "s_nop 1\n"
    "v_permlane16_swap_b32 v150, v151\n"
    "v_add_f32 v148, v150, v151\n"
    "s_nop 1\n"
    "v_add_f32_dpp v148, v148, v148 row_ror:4 row_mask:0xf bank_mask:0xf\n"
    "s_nop 1\n"
    "v_add_f32_dpp v148, v148, v148 row_ror:8 row_mask:0xf bank_mask:0xf\n"
    "ds_write_b32 v142, v148\n"
    "s_waitcnt lgkmcnt(0)\n"
    "Lwt_111:\n"
    "s_memrealtime s[78:79]\n"
    "s_waitcnt lgkmcnt(0)\n"
    "s_sub_u32 s78, s78, s11\n"
    "s_cmp_lt_u32 s78, 512\n"
    "s_cbranch_scc1 Lwt_111\n"
    "buffer_load_dwordx4 v[184:187], v113, s[12:15], 0 offen offset:0 sc1\n"
    "s_barrier\n"
    "ds_read_b128 v[152:155], v143\n"
    "ds_read_b128 v[156:159], v143 offset:64\n"
    "s_waitcnt lgkmcnt(1)\n"
    "v_pk_add_f32 v[152:153], v[152:153], v[154:155]\n"
    "v_add_f32 v152, v152, v153\n"
    "s_waitcnt lgkmcnt(0)\n"
    "v_pk_add_f32 v[156:157], v[156:157], v[158:159]\n"
    "v_add_f32 v156, v156, v157\n"
    "v_mul_f32 v6, 0x3b000000, v156\n"
    "v_mul_f32 v153, 0x3b000000, v152\n"
    "v_fma_f32 v153, -v6, v6, v153\n"
    "v_max_f32 v153, 0, v153\n"
    "v_add_f32 v153, 0x3727c5ac, v153\n"
    "v_rsq_f32 v5, v153\n"
    "s_nop 0\n"
    "v_mul_f32 v5, 0x3e0293ee, v5\n"
    "v_mul_f32 v6, v6, v4\n"
    "v_mul_f32 v6, v6, v5\n"
    "Lhq1c_112:\n"
    "s_waitcnt vmcnt(0)\n"
    "v_mfma_f32_16x16x32_bf16 v[200:203], v[184:187], v[32:35], 0\n"
    "v_mfma_f32_16x16x32_bf16 v[208:211], v[184:187], v[40:43], 0\n"
    "v_mfma_f32_16x16x32_bf16 v[204:207], v[184:187], v[36:39], 0\n"
    "v_mfma_f32_16x16x32_bf16 v[212:215], v[184:187], v[44:47], 0\n"
    "s_nop 5\n"
    "v_permlane32_swap_b32 v200, v208\n"
    "v_permlane32_swap_b32 v201, v209\n"
    "v_permlane32_swap_b32 v202, v210\n"
    "v_permlane32_swap_b32 v203, v211\n"
    "v_permlane32_swap_b32 v204, v212\n"
    "v_permlane32_swap_b32 v205, v213\n"
    "v_permlane32_swap_b32 v206, v214\n"
    "v_permlane32_swap_b32 v207, v215\n"
    "v_add_f32 v200, v200, v208\n"
    "v_add_f32 v201, v201, v209\n"
    "v_add_f32 v202, v202, v210\n"
    "v_add_f32 v203, v203, v211\n"
    "v_add_f32 v204, v204, v212\n"
    "v_add_f32 v205, v205, v213\n"
    "v_add_f32 v206, v206, v214\n"
    "v_add_f32 v207, v207, v215\n"
    "v_permlane16_swap_b32 v200, v204\n"
    "v_permlane16_swap_b32 v201, v205\n"
    "v_permlane16_swap_b32 v202, v206\n"
    "v_permlane16_swap_b32 v203, v207\n"
    "v_add_f32 v200, v200, v204\n"
    "v_add_f32 v201, v201, v205\n"
    "ds_write2st64_b32 v139, v200, v201 offset0:0 offset1:1\n"
    "v_add_f32 v202, v202, v206\n"
    "v_add_f32 v203, v203, v207\n"
    "ds_write2st64_b32 v139, v202, v203 offset0:2 offset1:3\n"
    "v_add_f32 v204, v200, v201\n"
    "v_add_f32 v205, v202, v203\n"
    "v_cmp_u_f32 vcc, v204, v205\n"
    "s_cbranch_vccnz Lhq1s_113\n"
    "Lhq1d_114:\n"
    "s_waitcnt lgkmcnt(0)\n"
    "s_barrier\n"
    "ds_read2st64_b32 v[184:185], v141 offset0:0 offset1:4\n"
    "ds_read2st64_b32 v[186:187], v141 offset0:8 offset1:12\n"
    "s_waitcnt lgkmcnt(0)\n"
    "v_pk_add_f32 v[184:185], v[184:185], v[186:187]\n"
    "v_add_f32 v184, v184, v185\n"
    "v_fma_f32 v184, v184, v5, -v6\n"
    "s_nop 1\n"
    "v_mov_b32_dpp v185, v184 quad_perm:[1,1,3,3] row_mask:0xf bank_mask:0xf\n"
    "v_cvt_pk_bf16_f32 v184, v184, v185\n"
    "buffer_store_dword v184, v115, s[12:15], 0 offen sc1\n"
    "Lseg_B1_0:\n"
    "s_cbranch_execz Lseg_B1_1\n"
    "s_mul_i32 s84, s65, 2112\n"
    "s_add_u32 s93, s98, s84\n"
    "s_sub_u32 s94, s98, s84\n"
    "Lwt_115:\n"
    "s_memrealtime s[78:79]\n"
    "s_waitcnt lgkmcnt(0)\n"
    "s_sub_u32 s78, s78, s11\n"
    "s_cmp_lt_u32 s78, 580\n"
    "s_cbranch_scc1 Lwt_115\n"
    "s_add_u32 m0, s93, 135168\n"
    "s_nop 0\n"
    "buffer_load_dwordx4 v116, s[12:15], s83 offen offset:0 sc1 lds\n"
    "s_add_u32 m0, s93, 135200\n"
    "s_nop 0\n"
    "buffer_load_dwordx4 v116, s[12:15], s83 offen offset:1024 sc1 lds\n"
    "s_add_u32 m0, s94, 135232\n"
    "s_nop 0\n"
    "buffer_load_dwordx4 v116, s[12:15], s83 offen offset:2048 sc1 lds\n"
    "s_add_u32 m0, s94, 135264\n"
    "s_nop 0\n"
    "buffer_load_dwordx4 v116, s[12:15], s83 offen offset:3072 sc1 lds\n"
    "Lh11c_116:\n"
    "s_waitcnt vmcnt(0)\n"
    "s_barrier\n"
    "ds_read_b128 v[144:147], v124 offset:0\n"
    "ds_read_b128 v[148:151], v124 offset:64\n"
    "ds_read_b128 v[152:155], v124 offset:128\n"
    "ds_read_b128 v[156:159], v124 offset:192\n"
    "s_waitcnt lgkmcnt(3)\n"
    "v_mfma_f32_16x16x32_bf16 v[208:211], a[0:3], v[144:147], 0\n"
    "v_mfma_f32_16x16x32_bf16 v[212:215], a[64:67], v[144:147], 0\n"
    "ds_read_b128 v[160:163], v124 offset:256\n"
    "ds_read_b128 v[164:167], v124 offset:320\n"
    "ds_read_b128 v[168:171], v124 offset:384\n"
    "s_waitcnt lgkmcnt(5)\n"
    "v_mfma_f32_16x16x32_bf16 v[208:211], a[4:7], v[148:151], v[208:211]\n"
    "v_mfma_f32_16x16x32_bf16 v[212:215], a[68:71], v[148:151], v[212:215]\n"
    "ds_read_b128 v[172:175], v124 offset:448\n"
    "ds_read_b128 v[176:179], v124 offset:512\n"
    "ds_read_b128 v[180:183], v124 offset:576\n"
    "s_waitcnt lgkmcnt(7)\n"
    "v_mfma_f32_16x16x32_bf16 v[208:211], a[8:11], v[152:155], v[208:211]\n"
    "v_mfma_f32_16x16x32_bf16 v[212:215], a[72:75], v[152:155], v[212:215]\n"
    "ds_read_b128 v[184:187], v124 offset:640\n"
    "ds_read_b128 v[188:191], v124 offset:704\n"
    "ds_read_b128 v[192:195], v124 offset:768\n"
    "s_waitcnt lgkmcnt(9)\n"
    "v_mfma_f32_16x16x32_bf16 v[208:211], a[12:15], v[156:159], v[208:211]\n"
    "v_mfma_f32_16x16x32_bf16 v[212:215], a[76:79], v[156:159], v[212:215]\n"
    "ds_read_b128 v[196:199], v124 offset:832\n"
    "ds_read_b128 v[200:203], v124 offset:896\n"
    "ds_read_b128 v[204:207], v124 offset:960\n"
    "s_waitcnt lgkmcnt(11)\n"
    "v_mfma_f32_16x16x32_bf16 v[208:211], a[16:19], v[160:163], v[208:211]\n"
    "v_mfma_f32_16x16x32_bf16 v[212:215], a[80:83], v[160:163], v[212:215]\n"
    "s_waitcnt lgkmcnt(10)\n"
    "v_mfma_f32_16x16x32_bf16 v[208:211], a[20:23], v[164:167], v[208:211]\n"
    "v_mfma_f32_16x16x32_bf16 v[212:215], a[84:87], v[164:167], v[212:215]\n"
    "s_waitcnt lgkmcnt(9)\n"
    "v_mfma_f32_16x16x32_bf16 v[208:211], a[24:27], v[168:171], v[208:211]\n"
    "v_mfma_f32_16x16x32_bf16 v[212:215], a[88:91], v[168:171], v[212:215]\n"
    "s_waitcnt lgkmcnt(8)\n"
    "v_mfma_f32_16x16x32_bf16 v[208:211], a[28:31], v[172:175], v[208:211]\n"
    "v_mfma_f32_16x16x32_bf16 v[212:215], a[92:95], v[172:175], v[212:215]\n"
    "s_waitcnt lgkmcnt(7)\n"
    "v_mfma_f32_16x16x32_bf16 v[208:211], a[32:35], v[176:179], v[208:211]\n"
    "v_mfma_f32_16x16x32_bf16 v[212:215], a[96:99], v[176:179], v[212:215]\n"
    "s_waitcnt lgkmcnt(6)\n"
    "v_mfma_f32_16x16x32_bf16 v[208:211], a[36:39], v[180:183], v[208:211]\n"
    "v_mfma_f32_16x16x32_bf16 v[212:215], a[100:103], v[180:183], v[212:215]\n"
    "s_waitcnt lgkmcnt(5)\n"
    "v_mfma_f32_16x16x32_bf16 v[208:211], a[40:43], v[184:187], v[208:211]\n"
    "v_mfma_f32_16x16x32_bf16 v[212:215], a[104:107], v[184:187], v[212:215]\n"
    "s_waitcnt lgkmcnt(4)\n"
    "v_mfma_f32_16x16x32_bf16 v[208:211], a[44:47], v[188:191], v[208:211]\n"
    "v_mfma_f32_16x16x32_bf16 v[212:215], a[108:111], v[188:191], v[212:215]\n"
    "s_waitcnt lgkmcnt(3)\n"
    "v_mfma_f32_16x16x32_bf16 v[208:211], a[48:51], v[192:195], v[208:211]\n"
    "v_mfma_f32_16x16x32_bf16 v[212:215], a[112:115], v[192:195], v[212:215]\n"
    "s_waitcnt lgkmcnt(2)\n"
    "v_mfma_f32_16x16x32_bf16 v[208:211], a[52:55], v[196:199], v[208:211]\n"
    "v_mfma_f32_16x16x32_bf16 v[212:215], a[116:119], v[196:199], v[212:215]\n"
    "s_waitcnt lgkmcnt(1)\n"
    "v_mfma_f32_16x16x32_bf16 v[208:211], a[56:59], v[200:203], v[208:211]\n"
    "v_mfma_f32_16x16x32_bf16 v[212:215], a[120:123], v[200:203], v[212:215]\n"
    "s_waitcnt lgkmcnt(0)\n"
    "v_mfma_f32_16x16x32_bf16 v[208:211], a[60:63], v[204:207], v[208:211]\n"
    "v_mfma_f32_16x16x32_bf16 v[212:215], a[124:127], v[204:207], v[212:215]\n"
    "v_mov_b32 v152, 0x3f803f80\n"
    "v_mov_b32 v153, 0x3f803f80\n"
    "v_mov_b32 v154, 0x3f803f80\n"
    "v_mov_b32 v155, 0x3f803f80\n"
    "s_nop 2\n"
    "v_max3_f32 v144, v208, v209, v210\n"
    "v_max3_f32 v145, v211, v212, v213\n"
    "v_max3_f32 v145, v145, v214, v215\n"
    "v_cmp_u_f32 vcc, v208, v212\n"
    "v_max_f32 v144, v144, v145\n"
    "v_max_f32 v145, v144, v145\n"
    "s_cbranch_vccnz Lh11s_117\n"
    "Lh11d_118:\n"
    "s_nop 0\n"
    "v_permlane32_swap_b32 v144, v145\n"
    "v_max_f32 v146, v144, v145\n"
    "v_max_f32 v147, v144, v145\n"
    "s_nop 1\n"
    "v_permlane16_swap_b32 v146, v147\n"
    "v_max_f32 v144, v146, v147\n"
    "ds_write_b32 v125, v144\n"
    "s_waitcnt lgkmcnt(0)\n"
    "s_barrier\n"
    "ds_read_b128 v[148:151], v126\n"
    "s_waitcnt lgkmcnt(0)\n"
    "v_max3_f32 v146, v148, v149, v150\n"
    "v_max_f32 v146, v146, v151\n"
    "v_sub_f32 v208, v208, v146\n"
    "v_sub_f32 v209, v209, v146\n"
    "v_exp_f32 v208, v208\n"
    "v_sub_f32 v210, v210, v146\n"
    "v_exp_f32 v209, v209\n"
    "v_sub_f32 v211, v211, v146\n"
    "v_exp_f32 v210, v210\n"
    "v_sub_f32 v212, v212, v146\n"
    "v_exp_f32 v211, v211\n"
    "v_sub_f32 v213, v213, v146\n"
    "v_exp_f32 v212, v212\n"
    "v_sub_f32 v214, v214, v146\n"
    "v_exp_f32 v213, v213\n"
    "v_sub_f32 v215, v215, v146\n"
    "v_exp_f32 v214, v214\n"
    "v_exp_f32 v215, v215\n"
    "v_cvt_pk_bf16_f32 v216, v208, v209\n"
    "v_cvt_pk_bf16_f32 v217, v210, v211\n"
    "v_cvt_pk_bf16_f32 v218, v212, v213\n"
    "v_cvt_pk_bf16_f32 v219, v214, v215\n"
    "ds_write_b128 v127, v[216:219]\n"
    "s_nop 0\n"
    "v_mfma_f32_16x16x32_bf16 v[168:171], v[216:219], a[128:131], 0\n"
    "v_mfma_f32_16x16x32_bf16 v[172:175], v[216:219], a[144:147], 0\n"
    "v_mfma_f32_16x16x32_bf16 v[176:179], v[216:219], a[160:163], 0\n"
    "s_waitcnt lgkmcnt(0)\n"
    "s_barrier\n"
    "ds_read_b128 v[156:159], v128\n"
    "ds_read_b128 v[160:163], v129\n"
    "ds_read_b128 v[164:167], v130\n"
    "v_mfma_f32_16x16x32_bf16 v[180:183], v[216:219], a[176:179], 0\n"
    "v_mfma_f32_16x16x32_bf16 v[184:187], v[216:219], a[192:195], 0\n"
    "v_mfma_f32_16x16x32_bf16 v[188:191], v[216:219], a[208:211], 0\n"
    "v_mfma_f32_16x16x32_bf16 v[192:195], v[216:219], a[224:227], 0\n"
    "v_mfma_f32_16x16x32_bf16 v[196:199], v[216:219], a[240:243], 0\n"
    "v_mfma_f32_16x16x32_bf16 v[200:203], v[152:155], v[216:219], 0\n"
    "s_waitcnt lgkmcnt(2)\n"
    "v_mfma_f32_16x16x32_bf16 v[200:203], v[152:155], v[156:159], v[200:203]\n"
    "s_waitcnt lgkmcnt(1)\n"
    "v_mfma_f32_16x16x32_bf16 v[200:203], v[152:155], v[160:163], v[200:203]\n"
    "s_waitcnt lgkmcnt(0)\n"
    "v_mfma_f32_16x16x32_bf16 v[200:203], v[152:155], v[164:167], v[200:203]\n"
    "Lseg_B1_1:\n"
    "s_cbranch_execz Ltret\n"
    "v_mfma_f32_16x16x32_bf16 v[168:171], v[156:159], a[132:135], v[168:171]\n"
    "v_mfma_f32_16x16x32_bf16 v[172:175], v[156:159], a[148:151], v[172:175]\n"
    "v_mfma_f32_16x16x32_bf16 v[176:179], v[156:159], a[164:167], v[176:179]\n"
    "v_mfma_f32_16x16x32_bf16 v[180:183], v[156:159], a[180:183], v[180:183]\n"
    "s_nop 3\n"
    "v_mov_b32 v147, v200\n"
    "buffer_store_dwordx2 v[146:147], v119, s[12:15], s83 offen sc1\n"
    "v_mfma_f32_16x16x32_bf16 v[168:171], v[160:163], a[136:139], v[168:171]\n"
    "v_mfma_f32_16x16x32_bf16 v[172:175], v[160:163], a[152:155], v[172:175]\n"
    "v_mfma_f32_16x16x32_bf16 v[176:179], v[160:163], a[168:171], v[176:179]\n"
    "v_mfma_f32_16x16x32_bf16 v[180:183], v[160:163], a[184:187], v[180:183]\n"
    "v_mfma_f32_16x16x32_bf16 v[168:171], v[164:167], a[140:143], v[168:171]\n"
    "v_mfma_f32_16x16x32_bf16 v[172:175], v[164:167], a[156:159], v[172:175]\n"
    "v_mfma_f32_16x16x32_bf16 v[176:179], v[164:167], a[172:175], v[176:179]\n"
    "v_mfma_f32_16x16x32_bf16 v[180:183], v[164:167], a[188:191], v[180:183]\n"
    "v_mfma_f32_16x16x32_bf16 v[184:187], v[156:159], a[196:199], v[184:187]\n"
    "v_mfma_f32_16x16x32_bf16 v[188:191], v[156:159], a[212:215], v[188:191]\n"
    "v_mfma_f32_16x16x32_bf16 v[192:195], v[156:159], a[228:231], v[192:195]\n"
    "s_nop 2\n"
    "v_cvt_pk_bf16_f32 v204, v168, v172\n"
    "s_nop 0\n"
    "v_cvt_pk_bf16_f32 v205, v176, v180\n"
    "buffer_store_dwordx2 v[204:205], v117, s[12:15], s70 offen sc1\n"
    "v_mfma_f32_16x16x32_bf16 v[196:199], v[156:159], a[244:247], v[196:199]\n"
    "v_mfma_f32_16x16x32_bf16 v[184:187], v[160:163], a[200:203], v[184:187]\n"
    "v_cvt_pk_bf16_f32 v206, v169, v173\n"
    "v_cvt_pk_bf16_f32 v207, v177, v181\n"
    "buffer_store_dwordx2 v[206:207], v117, s[12:15], s71 offen sc1\n"
    "v_mfma_f32_16x16x32_bf16 v[188:191], v[160:163], a[216:219], v[188:191]\n"
    "v_mfma_f32_16x16x32_bf16 v[192:195], v[160:163], a[232:235], v[192:195]\n"
    "v_cvt_pk_bf16_f32 v208, v170, v174\n"
    "v_cvt_pk_bf16_f32 v209, v178, v182\n"
    "buffer_store_dwordx2 v[208:209], v117, s[12:15], s72 offen sc1\n"
    "v_mfma_f32_16x16x32_bf16 v[196:199], v[160:163], a[248:251], v[196:199]\n"
    "v_mfma_f32_16x16x32_bf16 v[184:187], v[164:167], a[204:207], v[184:187]\n"
    "v_cvt_pk_bf16_f32 v210, v171, v175\n"
    "v_cvt_pk_bf16_f32 v211, v179, v183\n"
    "buffer_store_dwordx2 v[210:211], v117, s[12:15], s73 offen sc1\n"
    "v_mfma_f32_16x16x32_bf16 v[188:191], v[164:167], a[220:223], v[188:191]\n"
    "v_mfma_f32_16x16x32_bf16 v[192:195], v[164:167], a[236:239], v[192:195]\n"
    "v_mfma_f32_16x16x32_bf16 v[196:199], v[164:167], a[252:255], v[196:199]\n"
    "s_nop 5\n"
    "v_cvt_pk_bf16_f32 v220, v184, v188\n"
    "s_nop 0\n"
    "v_cvt_pk_bf16_f32 v221, v192, v196\n"
    "buffer_store_dwordx2 v[220:221], v117, s[12:15], s74 offen sc1\n"
    "v_cvt_pk_bf16_f32 v222, v185, v189\n"
    "v_cvt_pk_bf16_f32 v223, v193, v197\n"
    "buffer_store_dwordx2 v[222:223], v117, s[12:15], s75 offen sc1\n"
    "buffer_load_dwordx2 v[144:145], v120, s[12:15], s83 offen sc1\n"
    "v_cvt_pk_bf16_f32 v224, v186, v190\n"
    "v_cvt_pk_bf16_f32 v225, v194, v198\n"
    "buffer_store_dwordx2 v[224:225], v117, s[12:15], s76 offen sc1\n"
    "v_cvt_pk_bf16_f32 v226, v187, v191\n"
    "v_cvt_pk_bf16_f32 v227, v195, v199\n"
    "buffer_store_dwordx2 v[226:227], v117, s[12:15], s77 offen sc1\n"
    "s_bitcmp1_b32 s61, 0\n"
    "s_cbranch_scc1 Lfc_119\n"
    "Lseg_C1_0:\n"
    "s_cbranch_execz Lseg_C1_1\n"
    "s_lshl_b32 s5, s59, 13\n"
    "s_bfe_u32 s85, s61, 0x20001\n"
    "s_xor_b32 s85, s85, 0\n"
    "s_lshl_b32 s88, s85, 10\n"
    "s_add_u32 s92, s88, 32768\n"
    "s_bfe_u32 s85, s61, 0x20001\n"
    "s_xor_b32 s85, s85, 1\n"
    "s_lshl_b32 s89, s85, 10\n"
    "s_add_u32 s93, s89, 32768\n"
    "s_bfe_u32 s85, s61, 0x20001\n"
    "s_xor_b32 s85, s85, 2\n"
    "s_lshl_b32 s90, s85, 10\n"
    "s_add_u32 s94, s90, 32768\n"
    "s_bfe_u32 s85, s61, 0x20001\n"
    "s_xor_b32 s85, s85, 3\n"
    "s_lshl_b32 s91, s85, 10\n"
    "s_add_u32 s95, s91, 32768\n"
    "Lwt_122:\n"
    "s_memrealtime s[78:79]\n"
    "s_waitcnt lgkmcnt(0)\n"
    "s_sub_u32 s78, s78, s11\n"
    "s_cmp_lt_u32 s78, 740\n"
    "s_cbranch_scc1 Lwt_122\n"
    "s_add_u32 m0, s5, 67584\n"
    "s_nop 0\n"
    "buffer_load_dwordx4 v118, s[12:15], s88 offen offset:0 sc1 lds\n"
    "s_add_u32 m0, s5, 68608\n"
    "s_nop 0\n"
    "buffer_load_dwordx4 v118, s[12:15], s89 offen offset:0 sc1 lds\n"
    "s_sleep 2\n"
    "s_add_u32 m0, s5, 69632\n"
    "s_nop 0\n"
    "buffer_load_dwordx4 v118, s[12:15], s90 offen offset:0 sc1 lds\n"
    "s_add_u32 m0, s5, 70656\n"
    "s_nop 0\n"
    "buffer_load_dwordx4 v118, s[12:15], s91 offen offset:0 sc1 lds\n"
    "s_sleep 2\n"
    "s_add_u32 m0, s5, 71680\n"
    "s_nop 0\n"
    "buffer_load_dwordx4 v118, s[12:15], s92 offen offset:0 sc1 lds\n"
    "s_add_u32 m0, s5, 72704\n"
    "s_nop 0\n"
    "buffer_load_dwordx4 v118, s[12:15], s93 offen offset:0 sc1 lds\n"
    "s_sleep 2\n"
    "s_add_u32 m0, s5, 73728\n"
    "s_nop 0\n"
    "buffer_load_dwordx4 v118, s[12:15], s94 offen offset:0 sc1 lds\n"
    "s_add_u32 m0, s5, 74752\n"
    "s_nop 0\n"
    "buffer_load_dwordx4 v118, s[12:15], s95 offen offset:0 sc1 lds\n"
    "Lh21c_123:\n"
    "s_waitcnt vmcnt(10)\n"
    "v_max_f32_dpp v146, v144, v144 row_ror:1 row_mask:0xf bank_mask:0xf\n"
    "s_nop 1\n"
    "v_max_f32_dpp v146, v146, v146 row_ror:2 row_mask:0xf bank_mask:0xf\n"
    "s_nop 1\n"
    "v_max_f32_dpp v146, v146, v146 row_ror:4 row_mask:0xf bank_mask:0xf\n"
    "s_nop 1\n"
    "v_max_f32_dpp v146, v146, v146 row_ror:8 row_mask:0xf bank_mask:0xf\n"
    "v_mov_b32 v147, v146\n"
    "s_nop 1\n"
    "v_permlane16_swap_b32 v146, v147\n"
    "v_max_f32 v146, v146, v147\n"
    "v_sub_f32 v148, v144, v146\n"
    "v_exp_f32 v148, v148\n"
    "s_nop 0\n"
    "v_mul_f32 v150, v148, v145\n"
    "s_nop 1\n"
    "v_add_f32_dpp v149, v150, v150 row_ror:1 row_mask:0xf bank_mask:0xf\n"
    "Lseg_C1_1:\n"
    "s_cbranch_execz Lseg_C1_2\n"
    "s_nop 1\n"
    "v_add_f32_dpp v149, v149, v149 row_ror:2 row_mask:0xf bank_mask:0xf\n"
    "s_nop 1\n"
    "v_add_f32_dpp v149, v149, v149 row_ror:4 row_mask:0xf bank_mask:0xf\n"
    "s_nop 1\n"
    "v_add_f32_dpp v149, v149, v149 row_ror:8 row_mask:0xf bank_mask:0xf\n"
    "v_mov_b32 v147, v149\n"
    "s_nop 1\n"
    "v_permlane16_swap_b32 v149, v147\n"
    "v_add_f32 v149, v149, v147\n"
    "v_rcp_f32 v149, v149\n"
    "s_nop 0\n"
    "v_mul_f32 v148, v148, v149\n"
    "v_cvt_pk_bf16_f32 v148, v148, v148\n"
    "ds_write_b16 v135, v148\n"
    "ds_read_b128 v[152:155], v136\n"
    "s_waitcnt vmcnt(6)\n"
    "ds_read_b64_tr_b16 v[156:157], v131 offset:0\n"
    "ds_read_b64_tr_b16 v[160:161], v132 offset:0\n"
    "ds_read_b64_tr_b16 v[164:165], v133 offset:0\n"
    "ds_read_b64_tr_b16 v[168:169], v134 offset:0\n"
    "s_waitcnt vmcnt(4)\n"
    "ds_read_b64_tr_b16 v[158:159], v131 offset:2048\n"
    "ds_read_b64_tr_b16 v[162:163], v132 offset:2048\n"
    "ds_read_b64_tr_b16 v[166:167], v133 offset:2048\n"
    "ds_read_b64_tr_b16 v[170:171], v134 offset:2048\n"
    "s_waitcnt lgkmcnt(3)\n"
    "v_mfma_f32_16x16x32_bf16 v[188:191], v[156:159], v[152:155], 0\n"
    "s_waitcnt lgkmcnt(2)\n"
    "v_mfma_f32_16x16x32_bf16 v[192:195], v[160:163], v[152:155], 0\n"
    "s_waitcnt lgkmcnt(1)\n"
    "v_mfma_f32_16x16x32_bf16 v[196:199], v[164:167], v[152:155], 0\n"
    "s_waitcnt lgkmcnt(0)\n"
    "v_mfma_f32_16x16x32_bf16 v[200:203], v[168:171], v[152:155], 0\n"
    "s_nop 1\n"
    "v_cvt_pk_bf16_f32 v204, v188, v189\n"
    "v_cvt_pk_bf16_f32 v205, v190, v191\n"
    "v_cvt_pk_bf16_f32 v206, v192, v193\n"
    "v_cvt_pk_bf16_f32 v207, v194, v195\n"
    "s_mov_b32 exec_lo, 0x00010001\n"
    "s_mov_b32 exec_hi, 0x00010001\n"
    "ds_write_b128 v137, v[204:207] offset:0\n"
    "v_cvt_pk_bf16_f32 v208, v196, v197\n"
    "v_cvt_pk_bf16_f32 v209, v198, v199\n"
    "v_cvt_pk_bf16_f32 v210, v200, v201\n"
    "v_cvt_pk_bf16_f32 v211, v202, v203\n"
    "ds_write_b128 v137, v[208:211] offset:256\n"
    "s_mov_b64 exec, -1\n"
    "s_waitcnt vmcnt(2)\n"
    "ds_read_b64_tr_b16 v[172:173], v131 offset:4096\n"
    "ds_read_b64_tr_b16 v[176:177], v132 offset:4096\n"
    "ds_read_b64_tr_b16 v[180:181], v133 offset:4096\n"
    "ds_read_b64_tr_b16 v[184:185], v134 offset:4096\n"
    "s_waitcnt vmcnt(0)\n"
    "ds_read_b64_tr_b16 v[174:175], v131 offset:6144\n"
    "ds_read_b64_tr_b16 v[178:179], v132 offset:6144\n"
    "ds_read_b64_tr_b16 v[182:183], v133 offset:6144\n"
    "ds_read_b64_tr_b16 v[186:187], v134 offset:6144\n"
    "s_waitcnt lgkmcnt(3)\n"
    "v_mfma_f32_16x16x32_bf16 v[188:191], v[172:175], v[152:155], 0\n"
    "s_waitcnt lgkmcnt(2)\n"
    "v_mfma_f32_16x16x32_bf16 v[192:195], v[176:179], v[152:155], 0\n"
    "s_waitcnt lgkmcnt(1)\n"
    "v_mfma_f32_16x16x32_bf16 v[196:199], v[180:183], v[152:155], 0\n"
    "s_waitcnt lgkmcnt(0)\n"
    "v_mfma_f32_16x16x32_bf16 v[200:203], v[184:187], v[152:155], 0\n"
    "s_nop 1\n"
    "v_cvt_pk_bf16_f32 v204, v188, v189\n"
    "v_cvt_pk_bf16_f32 v205, v190, v191\n"
    "v_cvt_pk_bf16_f32 v206, v192, v193\n"
    "v_cvt_pk_bf16_f32 v207, v194, v195\n"
    "s_mov_b32 exec_lo, 0x00010001\n"
    "s_mov_b32 exec_hi, 0x00010001\n"
    "ds_write_b128 v137, v[204:207] offset:512\n"
    "v_cvt_pk_bf16_f32 v208, v196, v197\n"
    "v_cvt_pk_bf16_f32 v209, v198, v199\n"
    "v_cvt_pk_bf16_f32 v210, v200, v201\n"
    "v_cvt_pk_bf16_f32 v211, v202, v203\n"
    "ds_write_b128 v137, v[208:211] offset:768\n"
    "s_mov_b64 exec, -1\n"
    "s_waitcnt lgkmcnt(0)\n"
    "s_barrier\n"
    "ds_read_b128 v[160:163], v138 offset:0\n"
    "ds_read_b128 v[164:167], v138 offset:256\n"
    "ds_read_b128 v[168:171], v138 offset:512\n"
    "ds_read_b128 v[172:175], v138 offset:768\n"
    "s_waitcnt lgkmcnt(3)\n"
    "v_mfma_f32_16x16x32_bf16 v[220:223], v[48:51], v[160:163], 0\n"
    "v_mfma_f32_16x16x32_bf16 v[224:227], v[64:67], v[160:163], 0\n"
    "s_waitcnt lgkmcnt(2)\n"
    "v_mfma_f32_16x16x32_bf16 v[220:223], v[52:55], v[164:167], v[220:223]\n"
    "v_mfma_f32_16x16x32_bf16 v[224:227], v[68:71], v[164:167], v[224:227]\n"
    "s_waitcnt lgkmcnt(1)\n"
    "v_mfma_f32_16x16x32_bf16 v[220:223], v[56:59], v[168:171], v[220:223]\n"
    "v_mfma_f32_16x16x32_bf16 v[224:227], v[72:75], v[168:171], v[224:227]\n"
    "s_waitcnt lgkmcnt(0)\n"
    "v_mfma_f32_16x16x32_bf16 v[220:223], v[60:63], v[172:175], v[220:223]\n"
    "v_mfma_f32_16x16x32_bf16 v[224:227], v[76:79], v[172:175], v[224:227]\n"
    "s_nop 6\n"
    "v_cmp_u_f32 vcc, v220, v220\n"
    "v_cvt_pk_bf16_f32 v156, v220, v221\n"
    "v_cvt_pk_bf16_f32 v157, v222, v223\n"
    "v_cvt_pk_bf16_f32 v158, v224, v225\n"
    "v_cvt_pk_bf16_f32 v159, v226, v227\n"
    "s_cbranch_vccnz Lh21s_124\n"
    "Lh21d_125:\n"
    "buffer_store_dwordx4 v[156:159], v123, s[12:15], s83 offen sc1\n"
    "s_bfe_u32 s85, s57, 0x20001\n"
    "s_lshl_b32 s84, s85, 14\n"
    "s_bfe_u32 s85, s57, 0x10000\n"
    "s_lshl_b32 s86, s85, 12\n"
    "s_add_u32 s84, s84, s86\n"
    "s_lshl_b32 s86, s97, 8\n"
    "s_add_u32 s84, s84, s86\n"
    "s_lshl_b32 s86, s59, 7\n"
    "s_add_u32 s84, s84, s86\n"
    "s_add_u32 s84, s84, s83\n"
    "s_add_u32 s95, s84, 0x20000\n"
    "Lwt_126:\n"
    "s_memrealtime s[78:79]\n"
    "s_waitcnt lgkmcnt(0)\n"
    "s_sub_u32 s78, s78, s11\n"
    "s_cmp_lt_u32 s78, 880\n"
    "s_cbranch_scc1 Lwt_126\n"
    "buffer_load_dwordx4 v[160:163], v114, s[12:15], s83 offen offset:0 sc1\n"
    "s_nop 11\n"
    "buffer_load_dwordx4 v[164:167], v114, s[12:15], s83 offen offset:1024 sc1\n"
    "s_nop 11\n"
    "buffer_load_dwordx4 v[168:171], v114, s[12:15], s83 offen offset:2048 sc1\n"
    "s_nop 11\n"
    "buffer_load_dwordx4 v[172:175], v114, s[12:15], s83 offen offset:3072 sc1\n"
    "Lh31c_127:\n"
    "s_waitcnt vmcnt(3)\n"
    "v_mfma_f32_16x16x32_bf16 v[176:179], v[160:163], v[80:83], 0\n"
    "v_mfma_f32_16x16x32_bf16 v[180:183], v[160:163], v[84:87], 0\n"
    "s_waitcnt vmcnt(2)\n"
    "v_mfma_f32_16x16x32_bf16 v[176:179], v[164:167], v[88:91], v[176:179]\n"
    "v_mfma_f32_16x16x32_bf16 v[180:183], v[164:167], v[92:95], v[180:183]\n"
    "s_waitcnt vmcnt(1)\n"
    "v_mfma_f32_16x16x32_bf16 v[176:179], v[168:171], v[96:99], v[176:179]\n"
    "v_mfma_f32_16x16x32_bf16 v[180:183], v[168:171], v[100:103], v[180:183]\n"
    "s_waitcnt vmcnt(0)\n"
    "v_mfma_f32_16x16x32_bf16 v[176:179], v[172:175], v[104:107], v[176:179]\n"
    "v_mfma_f32_16x16x32_bf16 v[180:183], v[172:175], v[108:111], v[180:183]\n"
    "s_nop 6\n"
    "v_permlane32_swap_b32 v176, v178\n"
    "v_permlane32_swap_b32 v177, v179\n"
    "v_permlane32_swap_b32 v180, v182\n"
    "v_permlane32_swap_b32 v181, v183\n"
    "v_add_f32 v176, v176, v178\n"
    "v_add_f32 v177, v177, v179\n"
    "v_add_f32 v180, v180, v182\n"
    "v_add_f32 v181, v181, v183\n"
    "v_permlane16_swap_b32 v176, v177\n"
    "s_nop 0\n"
    "v_permlane16_swap_b32 v180, v181\n"
    "v_add_f32 v176, v176, v177\n"
    "v_add_f32 v180, v180, v181\n"
    "ds_write2st64_b32 v139, v176, v180 offset0:0 offset1:1\n"
    "v_cmp_u_f32 vcc, v176, v176\n"
    "s_cbranch_vccnz Lh31s_128\n"
    "Lh31d_129:\n"
    "s_cmp_lt_u32 s59, 2\n"
    "s_waitcnt lgkmcnt(0)\n"
    "s_barrier\n"
    "s_cbranch_scc0 Lsk_130\n"
    "ds_read2st64_b32 v[160:161], v141 offset0:0 offset1:4\n"
    "ds_read2st64_b32 v[162:163], v141 offset0:8 offset1:12\n"
    "Lseg_C1_2:\n"
    "s_cbranch_execz Ltret\n"
    "s_waitcnt lgkmcnt(0)\n"
    "v_pk_add_f32 v[160:161], v[160:161], v[162:163]\n"
    "v_add_f32 v160, v160, v161\n"
    "s_nop 1\n"
    "v_mov_b32_dpp v161, v160 quad_perm:[0,0,2,2] row_mask:0xf bank_mask:0xf\n"
    "v_mov_b32_dpp v162, v160 quad_perm:[1,1,3,3] row_mask:0xf bank_mask:0xf\n"
    "v_cvt_pk_bf16_f32 v161, v161, v162\n"
    "buffer_store_dword v161, v121, s[12:15], s95 offen sc1\n"
    "Lsk_130:\n"
    "s_nop 0\n"
    "s_branch Ldj_121\n"
    "Lfc_119:\n"
    "s_cmp_ge_u32 s97, 14\n"
    "s_cbranch_scc1 Ld2_120\n"
    "s_mul_i32 s96, s57, 14\n"
    "s_add_u32 s96, s96, s97\n"
    "Lseg_D11_0:\n"
    "s_cbranch_execz Lseg_D11_1\n"
    "s_add_u32 s88, s83, 0x20000\n"
    "s_add_u32 s89, s83, 0x21000\n"
    "s_add_u32 s90, s83, 0x24000\n"
    "s_add_u32 s91, s83, 0x25000\n"
    "s_add_u32 s92, s83, 0x28000\n"
    "s_add_u32 s93, s83, 0x29000\n"
    "s_add_u32 s94, s83, 0x2c000\n"
    "s_add_u32 s95, s83, 0x2d000\n"
    "v_mov_b32 v188, 0x3f803f80\n"
    "v_mov_b32 v189, 0x3f803f80\n"
    "v_mov_b32 v190, 0x3f803f80\n"
    "v_mov_b32 v191, 0x3f803f80\n"
    "v_mov_b32 v192, 0x3f803f80\n"
    "v_mov_b32 v193, 0x3f803f80\n"
    "v_mov_b32 v194, 0x3f803f80\n"
    "v_mov_b32 v195, 0x3f803f80\n"
    "v_mov_b32 v196, 0x3f803f80\n"
    "v_mov_b32 v197, 0x3f803f80\n"
    "v_mov_b32 v198, 0x3f803f80\n"
    "v_mov_b32 v199, 0x3f803f80\n"
    "v_mov_b32 v200, 0x3f803f80\n"
    "v_mov_b32 v201, 0x3f803f80\n"
    "v_mov_b32 v202, 0x3f803f80\n"
    "v_mov_b32 v203, 0x3f803f80\n"
    "s_mul_i32 s84, s59, 1280\n"
    "s_add_u32 s87, s84, 0x21000\n"
    "v_mul_u32_u24 v230, 288, v3\n"
    "v_add_u32 v227, s87, v230\n"
    "v_lshl_add_u32 v227, v2, 4, v227\n"
    "s_mov_b32 s87, 0x21000\n"
    "v_mul_u32_u24 v230, 288, v9\n"
    "v_add_u32 v224, s87, v230\n"
    "v_lshl_add_u32 v224, v3, 4, v224\n"
    "v_lshl_add_u32 v224, v10, 2, v224\n"
    "s_mov_b32 s87, 0x21000\n"
    "v_mul_u32_u24 v230, 20, v3\n"
    "v_add_u32 v225, s87, v230\n"
    "s_mov_b32 s87, 0x21040\n"
    "v_lshl_add_u32 v226, v3, 2, s87\n"
    "s_bfe_u32 s85, s96, 0x10006\n"
    "s_lshl_b32 s84, s85, 14\n"
    "s_bfe_u32 s85, s96, 0x60000\n"
    "s_lshl_b32 s86, s85, 7\n"
    "s_add_u32 s84, s84, s86\n"
    "s_add_u32 s84, s84, s83\n"
    "s_add_u32 s6, s84, 0x30000\n"
    "Lwt_131:\n"
    "s_memrealtime s[78:79]\n"
    "s_waitcnt lgkmcnt(0)\n"
    "s_sub_u32 s78, s78, s11\n"
    "s_cmp_lt_u32 s78, 948\n"
    "s_cbranch_scc1 Lwt_131\n"
    "s_waitcnt vmcnt(2)\n"
    "buffer_load_dwordx4 v[144:147], v122, s[12:15], s88 offen sc1\n"
    "s_nop 11\n"
    "buffer_load_dwordx4 v[148:151], v122, s[12:15], s89 offen sc1\n"
    "s_nop 11\n"
    "buffer_load_dwordx4 v[152:155], v122, s[12:15], s90 offen sc1\n"
    "s_nop 11\n"
    "buffer_load_dwordx4 v[156:159], v122, s[12:15], s91 offen sc1\n"
    "s_nop 11\n"
    "buffer_load_dwordx4 v[160:163], v122, s[12:15], s92 offen sc1\n"
    "s_nop 11\n"
    "buffer_load_dwordx4 v[164:167], v122, s[12:15], s93 offen sc1\n"
    "s_nop 11\n"
    "buffer_load_dwordx4 v[168:171], v122, s[12:15], s94 offen sc1\n"
    "s_nop 11\n"
    "buffer_load_dwordx4 v[172:175], v122, s[12:15], s95 offen sc1\n"
    "Lh41c_132:\n"
    "v_lshlrev_b32 v176, 16, v12\n"
    "v_and_b32 v177, 0xffff0000, v12\n"
    "v_lshlrev_b32 v178, 16, v13\n"
    "v_and_b32 v179, 0xffff0000, v13\n"
    "v_lshlrev_b32 v180, 16, v14\n"
    "v_and_b32 v181, 0xffff0000, v14\n"
    "v_lshlrev_b32 v182, 16, v15\n"
    "v_and_b32 v183, 0xffff0000, v15\n"
    "s_waitcnt vmcnt(7)\n"
    "v_lshlrev_b32 v216, 16, v144\n"
    "v_and_b32 v217, 0xffff0000, v144\n"
    "v_lshlrev_b32 v218, 16, v145\n"
    "v_and_b32 v219, 0xffff0000, v145\n"
    "v_pk_add_f32 v[176:177], v[176:177], v[216:217]\n"
    "v_lshlrev_b32 v220, 16, v146\n"
    "v_and_b32 v221, 0xffff0000, v146\n"
    "v_pk_add_f32 v[178:179], v[178:179], v[218:219]\n"
    "v_lshlrev_b32 v222, 16, v147\n"
    "v_and_b32 v223, 0xffff0000, v147\n"
    "v_pk_add_f32 v[180:181], v[180:181], v[220:221]\n"
    "v_pk_add_f32 v[182:183], v[182:183], v[222:223]\n"
    "s_waitcnt vmcnt(6)\n"
    "v_lshlrev_b32 v216, 16, v148\n"
    "v_and_b32 v217, 0xffff0000, v148\n"
    "v_lshlrev_b32 v218, 16, v149\n"
    "v_and_b32 v219, 0xffff0000, v149\n"
    "v_pk_add_f32 v[176:177], v[176:177], v[216:217]\n"
    "v_lshlrev_b32 v220, 16, v150\n"
    "v_and_b32 v221, 0xffff0000, v150\n"
    "v_pk_add_f32 v[178:179], v[178:179], v[218:219]\n"
    "v_lshlrev_b32 v222, 16, v151\n"
    "v_and_b32 v223, 0xffff0000, v151\n"
    "v_pk_add_f32 v[180:181], v[180:181], v[220:221]\n"
    "v_pk_add_f32 v[182:183], v[182:183], v[222:223]\n"
    "s_waitcnt vmcnt(5)\n"
    "v_lshlrev_b32 v216, 16, v152\n"
    "v_and_b32 v217, 0xffff0000, v152\n"
    "v_lshlrev_b32 v218, 16, v153\n"
    "v_and_b32 v219, 0xffff0000, v153\n"
    "v_pk_add_f32 v[176:177], v[176:177], v[216:217]\n"
    "v_lshlrev_b32 v220, 16, v154\n"
    "v_and_b32 v221, 0xffff0000, v154\n"
    "v_pk_add_f32 v[178:179], v[178:179], v[218:219]\n"
    "v_lshlrev_b32 v222, 16, v155\n"
    "v_and_b32 v223, 0xffff0000, v155\n"
    "v_pk_add_f32 v[180:181], v[180:181], v[220:221]\n"
    "v_pk_add_f32 v[182:183], v[182:183], v[222:223]\n"
    "s_waitcnt vmcnt(4)\n"
    "v_lshlrev_b32 v216, 16, v156\n"
    "v_and_b32 v217, 0xffff0000, v156\n"
    "v_lshlrev_b32 v218, 16, v157\n"
    "v_and_b32 v219, 0xffff0000, v157\n"
    "v_pk_add_f32 v[176:177], v[176:177], v[216:217]\n"
    "v_lshlrev_b32 v220, 16, v158\n"
    "v_and_b32 v221, 0xffff0000, v158\n"
    "v_pk_add_f32 v[178:179], v[178:179], v[218:219]\n"
    "v_lshlrev_b32 v222, 16, v159\n"
    "v_and_b32 v223, 0xffff0000, v159\n"
    "v_pk_add_f32 v[180:181], v[180:181], v[220:221]\n"
    "v_pk_add_f32 v[182:183], v[182:183], v[222:223]\n"
    "s_waitcnt vmcnt(3)\n"
    "v_lshlrev_b32 v216, 16, v160\n"
    "v_and_b32 v217, 0xffff0000, v160\n"
    "v_lshlrev_b32 v218, 16, v161\n"
    "v_and_b32 v219, 0xffff0000, v161\n"
    "v_pk_add_f32 v[176:177], v[176:177], v[216:217]\n"
    "v_lshlrev_b32 v220, 16, v162\n"
    "v_and_b32 v221, 0xffff0000, v162\n"
    "v_pk_add_f32 v[178:179], v[178:179], v[218:219]\n"
    "v_lshlrev_b32 v222, 16, v163\n"
    "v_and_b32 v223, 0xffff0000, v163\n"
    "v_pk_add_f32 v[180:181], v[180:181], v[220:221]\n"
    "v_pk_add_f32 v[182:183], v[182:183], v[222:223]\n"
    "s_waitcnt vmcnt(2)\n"
    "v_lshlrev_b32 v216, 16, v164\n"
    "v_and_b32 v217, 0xffff0000, v164\n"
    "v_lshlrev_b32 v218, 16, v165\n"
    "v_and_b32 v219, 0xffff0000, v165\n"
    "v_pk_add_f32 v[176:177], v[176:177], v[216:217]\n"
    "v_lshlrev_b32 v220, 16, v166\n"
    "v_and_b32 v221, 0xffff0000, v166\n"
    "v_pk_add_f32 v[178:179], v[178:179], v[218:219]\n"
    "v_lshlrev_b32 v222, 16, v167\n"
    "v_and_b32 v223, 0xffff0000, v167\n"
    "v_pk_add_f32 v[180:181], v[180:181], v[220:221]\n"
    "Lseg_D11_1:\n"
    "s_cbranch_execz Ltret\n"
    "v_pk_add_f32 v[182:183], v[182:183], v[222:223]\n"
    "s_waitcnt vmcnt(1)\n"
    "v_lshlrev_b32 v216, 16, v168\n"
    "v_and_b32 v217, 0xffff0000, v168\n"
    "v_lshlrev_b32 v218, 16, v169\n"
    "v_and_b32 v219, 0xffff0000, v169\n"
    "v_pk_add_f32 v[176:177], v[176:177], v[216:217]\n"
    "v_lshlrev_b32 v220, 16, v170\n"
    "v_and_b32 v221, 0xffff0000, v170\n"
    "v_pk_add_f32 v[178:179], v[178:179], v[218:219]\n"
    "v_lshlrev_b32 v222, 16, v171\n"
    "v_and_b32 v223, 0xffff0000, v171\n"
    "v_pk_add_f32 v[180:181], v[180:181], v[220:221]\n"
    "v_pk_add_f32 v[182:183], v[182:183], v[222:223]\n"
    "s_waitcnt vmcnt(0)\n"
    "v_lshlrev_b32 v216, 16, v172\n"
    "v_and_b32 v217, 0xffff0000, v172\n"
    "v_lshlrev_b32 v218, 16, v173\n"
    "v_and_b32 v219, 0xffff0000, v173\n"
    "v_pk_add_f32 v[176:177], v[176:177], v[216:217]\n"
    "v_lshlrev_b32 v220, 16, v174\n"
    "v_and_b32 v221, 0xffff0000, v174\n"
    "v_pk_add_f32 v[178:179], v[178:179], v[218:219]\n"
    "v_lshlrev_b32 v222, 16, v175\n"
    "v_and_b32 v223, 0xffff0000, v175\n"
    "v_pk_add_f32 v[180:181], v[180:181], v[220:221]\n"
    "v_pk_add_f32 v[182:183], v[182:183], v[222:223]\n"
    "v_cvt_pk_bf16_f32 v184, v176, v177\n"
    "v_cvt_pk_bf16_f32 v185, v178, v179\n"
    "v_cvt_pk_bf16_f32 v186, v180, v181\n"
    "v_cvt_pk_bf16_f32 v187, v182, v183\n"
    "v_mov_b32_dpp v188, v184 quad_perm:[0,1,2,3] row_mask:0xf bank_mask:0x1\n"
    "v_mov_b32_dpp v189, v185 quad_perm:[0,1,2,3] row_mask:0xf bank_mask:0x1\n"
    "v_mov_b32_dpp v190, v186 quad_perm:[0,1,2,3] row_mask:0xf bank_mask:0x1\n"
    "v_mov_b32_dpp v191, v187 quad_perm:[0,1,2,3] row_mask:0xf bank_mask:0x1\n"
    "v_mov_b32_dpp v192, v184 row_shl:4 row_mask:0xf bank_mask:0x1\n"
    "v_mov_b32_dpp v193, v185 row_shl:4 row_mask:0xf bank_mask:0x1\n"
    "v_mov_b32_dpp v194, v186 row_shl:4 row_mask:0xf bank_mask:0x1\n"
    "v_mov_b32_dpp v195, v187 row_shl:4 row_mask:0xf bank_mask:0x1\n"
    "v_mfma_f32_16x16x32_bf16 v[212:215], v[188:191], v[188:191], 0\n"
    "v_mfma_f32_16x16x32_bf16 v[204:207], v[48:51], v[188:191], 0\n"
    "v_mfma_f32_16x16x32_bf16 v[208:211], v[64:67], v[188:191], 0\n"
    "v_mov_b32_dpp v196, v184 row_shl:8 row_mask:0xf bank_mask:0x1\n"
    "v_mov_b32_dpp v197, v185 row_shl:8 row_mask:0xf bank_mask:0x1\n"
    "v_mov_b32_dpp v198, v186 row_shl:8 row_mask:0xf bank_mask:0x1\n"
    "v_mov_b32_dpp v199, v187 row_shl:8 row_mask:0xf bank_mask:0x1\n"
    "v_mfma_f32_16x16x32_bf16 v[212:215], v[192:195], v[192:195], v[212:215]\n"
    "v_mfma_f32_16x16x32_bf16 v[204:207], v[52:55], v[192:195], v[204:207]\n"
    "v_mfma_f32_16x16x32_bf16 v[208:211], v[68:71], v[192:195], v[208:211]\n"
    "v_mov_b32_dpp v200, v184 row_shl:12 row_mask:0xf bank_mask:0x1\n"
    "v_mov_b32_dpp v201, v185 row_shl:12 row_mask:0xf bank_mask:0x1\n"
    "v_mov_b32_dpp v202, v186 row_shl:12 row_mask:0xf bank_mask:0x1\n"
    "v_mov_b32_dpp v203, v187 row_shl:12 row_mask:0xf bank_mask:0x1\n"
    "v_mfma_f32_16x16x32_bf16 v[212:215], v[196:199], v[196:199], v[212:215]\n"
    "v_mfma_f32_16x16x32_bf16 v[204:207], v[56:59], v[196:199], v[204:207]\n"
    "v_mfma_f32_16x16x32_bf16 v[208:211], v[72:75], v[196:199], v[208:211]\n"
    "v_mfma_f32_16x16x32_bf16 v[212:215], v[200:203], v[200:203], v[212:215]\n"
    "v_mfma_f32_16x16x32_bf16 v[204:207], v[60:63], v[200:203], v[204:207]\n"
    "v_mfma_f32_16x16x32_bf16 v[208:211], v[76:79], v[200:203], v[208:211]\n"
    "s_nop 5\n"
    "ds_write_b128 v227, v[212:215] offset:10240\n"
    "ds_write_b128 v227, v[204:207] offset:0\n"
    "ds_write_b128 v227, v[208:211] offset:5120\n"
    "v_cmp_u_f32 vcc, v204, v204\n"
    "s_cbranch_vccnz Lh41s_133\n"
    "Lh41d_134:\n"
    "s_cmp_lt_u32 s59, 1\n"
    "s_waitcnt lgkmcnt(0)\n"
    "s_barrier\n"
    "s_cbranch_scc0 Lsk_135\n"
    "ds_read2st64_b32 v[144:145], v225 offset0:40 offset1:45\n"
    "ds_read2st64_b32 v[146:147], v225 offset0:50 offset1:55\n"
    "ds_read2st64_b32 v[148:149], v226 offset0:40 offset1:45\n"
    "ds_read2st64_b32 v[150:151], v226 offset0:50 offset1:55\n"
    "ds_read2st64_b32 v[152:153], v224 offset0:0 offset1:5\n"
    "ds_read2st64_b32 v[154:155], v224 offset0:10 offset1:15\n"
    "ds_read2st64_b32 v[160:161], v224 offset0:20 offset1:25\n"
    "ds_read2st64_b32 v[162:163], v224 offset0:30 offset1:35\n"
    "s_waitcnt lgkmcnt(6)\n"
    "v_pk_add_f32 v[144:145], v[144:145], v[146:147]\n"
    "s_waitcnt lgkmcnt(4)\n"
    "v_pk_add_f32 v[148:149], v[148:149], v[150:151]\n"
    "v_add_f32 v144, v144, v145\n"
    "v_add_f32 v148, v148, v149\n"
    "v_mul_f32 v168, 0x3b000000, v148\n"
    "v_mul_f32 v170, 0x3b000000, v144\n"
    "v_fma_f32 v170, -v168, v168, v170\n"
    "s_waitcnt lgkmcnt(2)\n"
    "v_pk_add_f32 v[152:153], v[152:153], v[154:155]\n"
    "v_max_f32 v170, 0, v170\n"
    "v_add_f32 v152, v152, v153\n"
    "v_add_f32 v170, 0x3727c5ac, v170\n"
    "v_rsq_f32 v169, v170\n"
    "v_fma_f32 v152, -v168, v80, v152\n"
    "v_mul_f32 v153, 0xbfb8aa3b, v152\n"
    "v_mul_f32 v153, v169, v153\n"
    "s_waitcnt lgkmcnt(0)\n"
    "v_pk_add_f32 v[160:161], v[160:161], v[162:163]\n"
    "v_exp_f32 v153, v153\n"
    "v_add_f32 v160, v160, v161\n"
    "v_add_f32 v153, 1.0, v153\n"
    "v_fma_f32 v160, -v168, v81, v160\n"
    "v_rcp_f32 v153, v153\n"
    "v_mul_f32 v152, v169, v152\n"
    "v_mul_f32 v160, v169, v160\n"
    "v_mul_f32 v152, v152, v153\n"
    "v_mul_f32 v152, v152, v160\n"
    "s_nop 1\n"
    "v_mov_b32_dpp v153, v152 quad_perm:[0,0,2,2] row_mask:0xf bank_mask:0xf\n"
    "v_mov_b32_dpp v154, v152 quad_perm:[1,1,3,3] row_mask:0xf bank_mask:0xf\n"
    "v_cvt_pk_bf16_f32 v153, v153, v154\n"
    "buffer_store_dword v153, v121, s[12:15], s6 offen sc1\n"
    "Lsk_135:\n"
    "s_nop 0\n"
    "s_branch Ldj_121\n"
    "Ld2_120:\n"
    "s_lshl_b32 s96, s57, 2\n"
    "s_lshl_b32 s86, s64, 1\n"
    "s_add_u32 s96, s96, s86\n"
    "s_bfe_u32 s85, s59, 0x10000\n"
    "s_add_u32 s96, s96, s85\n"
    "s_mul_i32 s5, s59, 28672\n"
    "s_lshl_b32 s84, s96, 5\n"
    "v_lshl_add_u32 v206, v3, 10, s84\n"
    "v_lshl_add_u32 v206, v2, 1, v206\n"
    "Lseg_D21_0:\n"
    "s_cbranch_execz Lseg_D21_1\n"
    "s_add_u32 s88, s83, 0x20000\n"
    "s_add_u32 s89, s83, 0x21000\n"
    "s_add_u32 s90, s83, 0x24000\n"
    "s_add_u32 s91, s83, 0x25000\n"
    "s_add_u32 s92, s83, 0x28000\n"
    "s_add_u32 s93, s83, 0x29000\n"
    "s_add_u32 s94, s83, 0x2c000\n"
    "s_add_u32 s95, s83, 0x2d000\n"
    "v_and_b32 v194, 1, v1\n"
    "v_mul_u32_u24 v194, 8190, v194\n"
    "v_sub_u32 v204, v121, v194\n"
    "s_lshl_b32 s84, s96, 7\n"
    "s_add_u32 s84, s84, s68\n"
    "v_add_u32 v204, s84, v204\n"
    "v_and_b32 v194, 1, v9\n"
    "s_mul_i32 s6, s59, 28\n"
    "s_add_u32 s6, s6, 0\n"
    "s_bfe_u32 s85, s6, 0x60000\n"
    "s_lshl_b32 s84, s85, 7\n"
    "s_bfe_u32 s85, s6, 0x10006\n"
    "s_lshl_b32 s86, s85, 14\n"
    "s_add_u32 s84, s84, s86\n"
    "s_add_u32 s84, s84, s68\n"
    "s_add_u32 s87, s84, 0x30000\n"
    "v_lshl_add_u32 v200, v1, 4, s87\n"
    "s_mul_i32 s6, s59, 28\n"
    "s_add_u32 s6, s6, 8\n"
    "s_bfe_u32 s85, s6, 0x60000\n"
    "s_lshl_b32 s84, s85, 7\n"
    "s_bfe_u32 s85, s6, 0x10006\n"
    "s_lshl_b32 s86, s85, 14\n"
    "s_add_u32 s84, s84, s86\n"
    "s_add_u32 s84, s84, s68\n"
    "s_add_u32 s87, s84, 0x30000\n"
    "v_lshl_add_u32 v201, v1, 4, s87\n"
    "s_mul_i32 s6, s59, 28\n"
    "s_add_u32 s6, s6, 16\n"
    "s_bfe_u32 s85, s6, 0x60000\n"
    "s_lshl_b32 s84, s85, 7\n"
    "s_bfe_u32 s85, s6, 0x10006\n"
    "s_lshl_b32 s86, s85, 14\n"
    "s_add_u32 s84, s84, s86\n"
    "s_add_u32 s84, s84, s68\n"
    "s_add_u32 s87, s84, 0x30000\n"
    "v_lshl_add_u32 v202, v1, 4, s87\n"
    "s_mul_i32 s6, s59, 28\n"
    "s_add_u32 s6, s6, 24\n"
    "s_bfe_u32 s85, s6, 0x60000\n"
    "s_lshl_b32 s84, s85, 7\n"
    "s_bfe_u32 s85, s6, 0x10006\n"
    "s_lshl_b32 s86, s85, 14\n"
    "s_add_u32 s84, s84, s86\n"
    "s_add_u32 s84, s84, s68\n"
    "s_add_u32 s87, s84, 0x30000\n"
    "v_lshl_add_u32 v203, v3, 7, s87\n"
    "v_lshl_add_u32 v203, v194, 6, v203\n"
    "v_lshl_add_u32 v203, v10, 4, v203\n"
    "s_mul_i32 s84, s59, 1280\n"
    "s_add_u32 s87, s84, 0x21000\n"
    "v_mul_u32_u24 v230, 288, v3\n"
    "v_add_u32 v6, s87, v230\n"
    "v_lshl_add_u32 v6, v2, 4, v6\n"
    "s_mov_b32 s87, 0x21000\n"
    "v_mul_u32_u24 v230, 288, v9\n"
    "v_add_u32 v205, s87, v230\n"
    "v_lshl_add_u32 v205, v3, 4, v205\n"
    "v_lshl_add_u32 v205, v10, 2, v205\n"
    "s_bfe_u32 s85, s59, 0x10000\n"
    "s_mul_i32 s84, s85, 5120\n"
    "v_add_u32 v205, s84, v205\n"
    "s_lshl_b32 s84, s96, 7\n"
    "s_add_u32 s6, s84, 0xe8000\n"
    "v_lshl_add_u32 v194, v3, 11, s5\n"
    "v_lshl_add_u32 v194, v9, 6, v194\n"
    "v_lshl_add_u32 v194, v10, 3, v194\n"
    "s_add_u32 s87, s5, 0x6000\n"
    "v_lshl_add_u32 v195, v3, 10, s87\n"
    "v_lshl_add_u32 v195, v9, 6, v195\n"
    "v_lshl_add_u32 v195, v10, 3, v195\n"
    "buffer_load_ushort v193, v206, s[52:55], 0 offen\n"
    "v_lshl_or_b32 v206, v1, 31, v206\n"
    "ds_read_b64_tr_b16 v[208:209], v194 offset:16384\n"
    "ds_read_b64_tr_b16 v[210:211], v194 offset:16640\n"
    "ds_read_b64_tr_b16 v[212:213], v194 offset:16416\n"
    "ds_read_b64_tr_b16 v[214:215], v194 offset:16672\n"
    "ds_read_b64_tr_b16 v[216:217], v194 offset:16896\n"
    "ds_read_b64_tr_b16 v[218:219], v194 offset:17152\n"
    "ds_read_b64_tr_b16 v[220:221], v194 offset:16928\n"
    "ds_read_b64_tr_b16 v[222:223], v194 offset:17184\n"
    "ds_read_b64_tr_b16 v[224:225], v194 offset:17408\n"
    "ds_read_b64_tr_b16 v[226:227], v194 offset:17664\n"
    "ds_read_b64_tr_b16 v[228:229], v194 offset:17440\n"
    "ds_read_b64_tr_b16 v[230:231], v194 offset:17696\n"
    "ds_read_b64_tr_b16 v[232:233], v194 offset:17920\n"
    "ds_read_b64_tr_b16 v[234:235], v194 offset:18176\n"
    "ds_read_b64_tr_b16 v[236:237], v194 offset:17952\n"
    "ds_read_b64_tr_b16 v[238:239], v194 offset:18208\n"
    "ds_read_b64_tr_b16 v[240:241], v195 offset:0\n"
    "ds_read_b64_tr_b16 v[242:243], v195 offset:256\n"
    "ds_read_b64_tr_b16 v[244:245], v195 offset:32\n"
    "ds_read_b64_tr_b16 v[246:247], v195 offset:288\n"
    "ds_read_b64_tr_b16 v[248:249], v195 offset:512\n"
    "ds_read_b64_tr_b16 v[250:251], v195 offset:768\n"
    "ds_read_b64_tr_b16 v[252:253], v195 offset:544\n"
    "ds_read_b64_tr_b16 v[254:255], v195 offset:800\n"
    "s_waitcnt lgkmcnt(0)\n"
    "Lwt_136:\n"
    "s_memrealtime s[78:79]\n"
    "s_waitcnt lgkmcnt(0)\n"
    "s_sub_u32 s78, s78, s11\n"
    "s_cmp_lt_u32 s78, 956\n"
    "s_cbranch_scc1 Lwt_136\n"
    "s_waitcnt vmcnt(3)\n"
    "buffer_load_ushort v144, v204, s[12:15], s88 offen sc1\n"
    "buffer_load_ushort v145, v204, s[12:15], s89 offen sc1\n"
    "buffer_load_ushort v146, v204, s[12:15], s90 offen sc1\n"
    "buffer_load_ushort v147, v204, s[12:15], s91 offen sc1\n"
    "buffer_load_ushort v148, v204, s[12:15], s92 offen sc1\n"
    "buffer_load_ushort v149, v204, s[12:15], s93 offen sc1\n"
    "buffer_load_ushort v150, v204, s[12:15], s94 offen sc1\n"
    "buffer_load_ushort v151, v204, s[12:15], s95 offen sc1\n"
    "Lhr1c_137:\n"
    "v_mov_b32 v207, v7\n"
    "s_waitcnt vmcnt(7)\n"
    "v_lshlrev_b32 v193, 16, v144\n"
    "v_add_f32 v207, v207, v193\n"
    "s_waitcnt vmcnt(6)\n"
    "v_lshlrev_b32 v193, 16, v145\n"
    "v_add_f32 v207, v207, v193\n"
    "s_waitcnt vmcnt(5)\n"
    "v_lshlrev_b32 v193, 16, v146\n"
    "v_add_f32 v207, v207, v193\n"
    "s_waitcnt vmcnt(4)\n"
    "v_lshlrev_b32 v193, 16, v147\n"
    "v_add_f32 v207, v207, v193\n"
    "s_waitcnt vmcnt(3)\n"
    "v_lshlrev_b32 v193, 16, v148\n"
    "v_add_f32 v207, v207, v193\n"
    "s_waitcnt vmcnt(2)\n"
    "v_lshlrev_b32 v193, 16, v149\n"
    "v_add_f32 v207, v207, v193\n"
    "s_waitcnt vmcnt(1)\n"
    "v_lshlrev_b32 v193, 16, v150\n"
    "v_add_f32 v207, v207, v193\n"
    "s_waitcnt vmcnt(0)\n"
    "v_lshlrev_b32 v193, 16, v151\n"
    "v_add_f32 v207, v207, v193\n"
    "v_cmp_u_f32 vcc, v207, v207\n"
    "s_cbranch_vccnz Lhr1s_138\n"
    "Lhr1d_139:\n"
    "Lwt_140:\n"
    "s_memrealtime s[78:79]\n"
    "s_waitcnt lgkmcnt(0)\n"
    "s_sub_u32 s78, s78, s11\n"
    "s_cmp_lt_u32 s78, 1048\n"
    "s_cbranch_scc1 Lwt_140\n"
    "buffer_load_dwordx4 v[152:155], v200, s[12:15], s83 offen sc1\n"
    "s_nop 11\n"
    "buffer_load_dwordx4 v[156:159], v201, s[12:15], s83 offen sc1\n"
    "s_nop 11\n"
    "buffer_load_dwordx4 v[160:163], v202, s[12:15], s83 offen sc1\n"
    "s_nop 11\n"
    "buffer_load_dwordx4 v[164:167], v203, s[12:15], s83 offen sc1\n"
    "Lh51c_141:\n"
    "Lseg_D21_1:\n"
    "s_cbranch_execz Lseg_D21_2\n"
    "s_waitcnt vmcnt(3)\n"
    "v_mfma_f32_16x16x32_bf16 v[168:171], v[48:51], v[152:155], 0\n"
    "v_mfma_f32_16x16x32_bf16 v[172:175], v[56:59], v[152:155], 0\n"
    "v_mfma_f32_16x16x32_bf16 v[176:179], v[64:67], v[152:155], 0\n"
    "v_mfma_f32_16x16x32_bf16 v[180:183], v[72:75], v[152:155], 0\n"
    "v_mfma_f32_16x16x32_bf16 v[184:187], v[52:55], v[152:155], 0\n"
    "v_mfma_f32_16x16x32_bf16 v[188:191], v[60:63], v[152:155], 0\n"
    "v_mfma_f32_16x16x32_bf16 v[192:195], v[68:71], v[152:155], 0\n"
    "v_mfma_f32_16x16x32_bf16 v[196:199], v[76:79], v[152:155], 0\n"
    "s_waitcnt vmcnt(2)\n"
    "v_mfma_f32_16x16x32_bf16 v[168:171], v[80:83], v[156:159], v[168:171]\n"
    "v_mfma_f32_16x16x32_bf16 v[172:175], v[88:91], v[156:159], v[172:175]\n"
    "v_mfma_f32_16x16x32_bf16 v[176:179], v[96:99], v[156:159], v[176:179]\n"
    "v_mfma_f32_16x16x32_bf16 v[180:183], v[104:107], v[156:159], v[180:183]\n"
    "v_mfma_f32_16x16x32_bf16 v[184:187], v[84:87], v[156:159], v[184:187]\n"
    "v_mfma_f32_16x16x32_bf16 v[188:191], v[92:95], v[156:159], v[188:191]\n"
    "v_mfma_f32_16x16x32_bf16 v[192:195], v[100:103], v[156:159], v[192:195]\n"
    "v_mfma_f32_16x16x32_bf16 v[196:199], v[108:111], v[156:159], v[196:199]\n"
    "s_waitcnt vmcnt(1)\n"
    "v_mfma_f32_16x16x32_bf16 v[176:179], v[224:227], v[160:163], v[176:179]\n"
    "v_mfma_f32_16x16x32_bf16 v[180:183], v[232:235], v[160:163], v[180:183]\n"
    "v_mfma_f32_16x16x32_bf16 v[168:171], v[208:211], v[160:163], v[168:171]\n"
    "v_mfma_f32_16x16x32_bf16 v[172:175], v[216:219], v[160:163], v[172:175]\n"
    "s_waitcnt vmcnt(0)\n"
    "v_mfma_f32_16x16x32_bf16 v[168:171], v[240:243], v[164:167], v[168:171]\n"
    "Lseg_D21_2:\n"
    "s_cbranch_execz Ltret\n"
    "v_mfma_f32_16x16x32_bf16 v[172:175], v[248:251], v[164:167], v[172:175]\n"
    "v_mfma_f32_16x16x32_bf16 v[192:195], v[228:231], v[160:163], v[192:195]\n"
    "s_nop 1\n"
    "v_add_f32_dpp v176, v180, v176 row_shl:4 row_mask:0xf bank_mask:0xf bound_ctrl:1\n"
    "v_add_f32_dpp v177, v181, v177 row_shl:4 row_mask:0xf bank_mask:0xf bound_ctrl:1\n"
    "v_mfma_f32_16x16x32_bf16 v[196:199], v[236:239], v[160:163], v[196:199]\n"
    "v_add_f32_dpp v178, v182, v178 row_shl:4 row_mask:0xf bank_mask:0xf bound_ctrl:1\n"
    "v_add_f32_dpp v179, v183, v179 row_shl:4 row_mask:0xf bank_mask:0xf bound_ctrl:1\n"
    "v_mfma_f32_16x16x32_bf16 v[184:187], v[212:215], v[160:163], v[184:187]\n"
    "v_mfma_f32_16x16x32_bf16 v[188:191], v[220:223], v[160:163], v[188:191]\n"
    "v_cmp_u_f32 vcc, v168, v168\n"
    "v_add_f32_dpp v168, v176, v168 row_shl:8 row_mask:0xf bank_mask:0xf bound_ctrl:1\n"
    "v_mfma_f32_16x16x32_bf16 v[184:187], v[244:247], v[164:167], v[184:187]\n"
    "v_add_f32_dpp v169, v177, v169 row_shl:8 row_mask:0xf bank_mask:0xf bound_ctrl:1\n"
    "v_add_f32_dpp v170, v178, v170 row_shl:8 row_mask:0xf bank_mask:0xf bound_ctrl:1\n"
    "v_mfma_f32_16x16x32_bf16 v[188:191], v[252:255], v[164:167], v[188:191]\n"
    "v_add_f32_dpp v171, v179, v171 row_shl:8 row_mask:0xf bank_mask:0xf bound_ctrl:1\n"
    "v_add_f32_dpp v168, v172, v168 row_shl:4 row_mask:0xf bank_mask:0xf bound_ctrl:1\n"
    "v_add_f32_dpp v169, v173, v169 row_shl:4 row_mask:0xf bank_mask:0xf bound_ctrl:1\n"
    "v_add_f32_dpp v170, v174, v170 row_shl:4 row_mask:0xf bank_mask:0xf bound_ctrl:1\n"
    "v_add_f32_dpp v171, v175, v171 row_shl:4 row_mask:0xf bank_mask:0xf bound_ctrl:1\n"
    "ds_write_b128 v6, v[168:171]\n"
    "v_add_f32_dpp v192, v196, v192 row_shl:4 row_mask:0xf bank_mask:0xf bound_ctrl:1\n"
    "v_add_f32_dpp v193, v197, v193 row_shl:4 row_mask:0xf bank_mask:0xf bound_ctrl:1\n"
    "v_add_f32_dpp v194, v198, v194 row_shl:4 row_mask:0xf bank_mask:0xf bound_ctrl:1\n"
    "v_add_f32_dpp v195, v199, v195 row_shl:4 row_mask:0xf bank_mask:0xf bound_ctrl:1\n"
    "v_add_f32_dpp v184, v192, v184 row_shl:8 row_mask:0xf bank_mask:0xf bound_ctrl:1\n"
    "v_add_f32_dpp v185, v193, v185 row_shl:8 row_mask:0xf bank_mask:0xf bound_ctrl:1\n"
    "v_add_f32_dpp v186, v194, v186 row_shl:8 row_mask:0xf bank_mask:0xf bound_ctrl:1\n"
    "v_add_f32_dpp v187, v195, v187 row_shl:8 row_mask:0xf bank_mask:0xf bound_ctrl:1\n"
    "v_add_f32_dpp v184, v188, v184 row_shl:4 row_mask:0xf bank_mask:0xf bound_ctrl:1\n"
    "v_add_f32_dpp v185, v189, v185 row_shl:4 row_mask:0xf bank_mask:0xf bound_ctrl:1\n"
    "v_add_f32_dpp v186, v190, v186 row_shl:4 row_mask:0xf bank_mask:0xf bound_ctrl:1\n"
    "v_add_f32_dpp v187, v191, v187 row_shl:4 row_mask:0xf bank_mask:0xf bound_ctrl:1\n"
    "ds_write_b128 v6, v[184:187] offset:5120\n"
    "s_cbranch_vccnz Lh51s_142\n"
    "Lh51d_143:\n"
    "s_cmp_lt_u32 s59, 2\n"
    "s_waitcnt lgkmcnt(0)\n"
    "s_barrier\n"
    "s_cbranch_scc0 Lsk_144\n"
    "ds_read2st64_b32 v[152:153], v205 offset0:0 offset1:5\n"
    "ds_read2st64_b32 v[154:155], v205 offset0:10 offset1:15\n"
    "s_waitcnt lgkmcnt(1)\n"
    "v_add_f32 v152, v152, v153\n"
    "v_add_f32 v152, v152, v207\n"
    "s_waitcnt lgkmcnt(0)\n"
    "v_add_f32 v154, v154, v155\n"
    "v_add_f32 v152, v152, v154\n"
    "s_nop 1\n"
    "v_mov_b32_dpp v153, v152 quad_perm:[1,1,3,3] row_mask:0xf bank_mask:0xf\n"
    "v_cvt_pk_bf16_f32 v152, v152, v153\n"
    "buffer_store_dword v152, v206, s[52:55], 0 offen sc1\n"
    "Lsk_144:\n"
    "s_nop 0\n"
    "Ldj_121:\n"
    "s_nop 1\n"
    "s_endpgm\n"
    "Ltret:\n"
    "s_setpc_b64 s[0:1]\n"
    "Lgfs_2:\n"
    "s_nop 7\n"
    "s_nop 7\n"
    "s_sub_u32 s82, s82, 1\n"
    "s_cbranch_scc1 Lgfs_2_x\n"
    "buffer_load_dwordx4 v[152:155], v108, s[12:15], 0 offen sc1\n"
    "s_waitcnt vmcnt(0)\n"
    "s_branch Lgfc_1\n"
    "Lgfs_2_x:\n"
    "s_mov_b32 s82, 0\n"
    "s_branch Lgfd_3\n"
    "Lfls_6:\n"
    "s_nop 7\n"
    "s_nop 7\n"
    "s_sub_u32 s82, s82, 1\n"
    "s_cbranch_scc1 Lfls_6_x\n"
    "buffer_load_dwordx4 v[48:51], v107, s[12:15], 0 offen sc1\n"
    "s_waitcnt vmcnt(0)\n"
    "s_branch Lflc_5\n"
    "Lfls_6_x:\n"
    "s_mov_b32 s82, 0\n"
    "s_branch Lfld_7\n"
    "Lqps_10:\n"
    "s_nop 7\n"
    "s_nop 7\n"
    "s_sub_u32 s82, s82, 1\n"
    "s_cbranch_scc1 Lqps_10_x\n"
    "s_mov_b32 s87, 0xb0000\n"
    "s_add_u32 m0, s6, 135168\n"
    "s_nop 0\n"
    "buffer_load_dwordx4 v222, s[12:15], s87 offen offset:0 sc1 lds\n"
    "s_add_u32 m0, s6, 135200\n"
    "s_nop 0\n"
    "buffer_load_dwordx4 v222, s[12:15], s87 offen offset:1024 sc1 lds\n"
    "s_add_u32 m0, s5, 135232\n"
    "s_nop 0\n"
    "buffer_load_dwordx4 v222, s[12:15], s87 offen offset:2048 sc1 lds\n"
    "s_add_u32 m0, s5, 135264\n"
    "s_nop 0\n"
    "buffer_load_dwordx4 v222, s[12:15], s87 offen offset:3072 sc1 lds\n"
    "s_mov_b32 s87, 0xb1000\n"
    "s_add_u32 m0, s6, 139392\n"
    "s_nop 0\n"
    "buffer_load_dwordx4 v222, s[12:15], s87 offen offset:0 sc1 lds\n"
    "s_add_u32 m0, s6, 139424\n"
    "s_nop 0\n"
    "buffer_load_dwordx4 v222, s[12:15], s87 offen offset:1024 sc1 lds\n"
    "s_add_u32 m0, s5, 139456\n"
    "s_nop 0\n"
    "buffer_load_dwordx4 v222, s[12:15], s87 offen offset:2048 sc1 lds\n"
    "s_add_u32 m0, s5, 139488\n"
    "s_nop 0\n"
    "buffer_load_dwordx4 v222, s[12:15], s87 offen offset:3072 sc1 lds\n"
    "s_mov_b32 s87, 0xb4000\n"
    "s_add_u32 m0, s6, 143616\n"
    "s_nop 0\n"
    "buffer_load_dwordx4 v222, s[12:15], s87 offen offset:0 sc1 lds\n"
    "s_add_u32 m0, s6, 143648\n"
    "s_nop 0\n"
    "buffer_load_dwordx4 v222, s[12:15], s87 offen offset:1024 sc1 lds\n"
    "s_add_u32 m0, s5, 143680\n"
    "s_nop 0\n"
    "buffer_load_dwordx4 v222, s[12:15], s87 offen offset:2048 sc1 lds\n"
    "s_add_u32 m0, s5, 143712\n"
    "s_nop 0\n"
    "buffer_load_dwordx4 v222, s[12:15], s87 offen offset:3072 sc1 lds\n"
    "s_mov_b32 s87, 0xb5000\n"
    "s_add_u32 m0, s6, 147840\n"
    "s_nop 0\n"
    "buffer_load_dwordx4 v222, s[12:15], s87 offen offset:0 sc1 lds\n"
    "s_add_u32 m0, s6, 147872\n"
    "s_nop 0\n"
    "buffer_load_dwordx4 v222, s[12:15], s87 offen offset:1024 sc1 lds\n"
    "s_add_u32 m0, s5, 147904\n"
    "s_nop 0\n"
    "buffer_load_dwordx4 v222, s[12:15], s87 offen offset:2048 sc1 lds\n"
    "s_add_u32 m0, s5, 147936\n"
    "s_nop 0\n"
    "buffer_load_dwordx4 v222, s[12:15], s87 offen offset:3072 sc1 lds\n"
    "s_waitcnt vmcnt(0)\n"
    "s_branch Lqpc_9\n"
    "Lqps_10_x:\n"
    "s_mov_b32 s82, 0\n"
    "s_branch Lqpd_11\n"
    "Lhq0s_15:\n"
    "s_nop 7\n"
    "s_nop 7\n"
    "s_sub_u32 s82, s82, 1\n"
    "s_cbranch_scc1 Lhq0s_15_x\n"
    "buffer_load_dwordx4 v[144:147], v105, s[12:15], s92 offen offset:0 sc1\n"
    "buffer_load_dwordx4 v[148:151], v105, s[12:15], s92 offen offset:1024 sc1\n"
    "buffer_load_dwordx4 v[152:155], v105, s[12:15], s92 offen offset:2048 sc1\n"
    "buffer_load_dwordx4 v[156:159], v105, s[12:15], s92 offen offset:3072 sc1\n"
    "s_waitcnt vmcnt(0)\n"
    "s_branch Lhq0c_14\n"
    "Lhq0s_15_x:\n"
    "s_mov_b32 s82, 0\n"
    "s_branch Lhq0d_16\n"
    "Lh10s_19:\n"
    "s_nop 7\n"
    "s_nop 7\n"
    "s_sub_u32 s10, s10, 1\n"
    "s_cbranch_scc1 Lh10s_19_x\n"
    "s_waitcnt lgkmcnt(0)\n"
    "s_barrier\n"
    "s_add_u32 m0, s93, 135168\n"
    "s_nop 0\n"
    "buffer_load_dwordx4 v116, s[12:15], s83 offen offset:0 sc1 lds\n"
    "s_add_u32 m0, s93, 135200\n"
    "s_nop 0\n"
    "buffer_load_dwordx4 v116, s[12:15], s83 offen offset:1024 sc1 lds\n"
    "s_add_u32 m0, s94, 135232\n"
    "s_nop 0\n"
    "buffer_load_dwordx4 v116, s[12:15], s83 offen offset:2048 sc1 lds\n"
    "s_add_u32 m0, s94, 135264\n"
    "s_nop 0\n"
    "buffer_load_dwordx4 v116, s[12:15], s83 offen offset:3072 sc1 lds\n"
    "s_waitcnt vmcnt(0)\n"
    "s_barrier\n"
    "s_waitcnt vmcnt(0)\n"
    "s_branch Lh10c_18\n"
    "Lh10s_19_x:\n"
    "s_mov_b32 s10, 0\n"
    "s_branch Lh10d_20\n"
    "Lh20s_27:\n"
    "s_nop 7\n"
    "s_nop 7\n"
    "s_sub_u32 s82, s82, 1\n"
    "s_cbranch_scc1 Lh20s_27_x\n"
    "buffer_load_dwordx2 v[144:145], v120, s[12:15], s83 offen sc1\n"
    "s_waitcnt vmcnt(0)\n"
    "s_branch Lh20c_26\n"
    "Lh20s_27_x:\n"
    "s_mov_b32 s82, 0\n"
    "s_branch Lh20d_28\n"
    "Lbcs_30:\n"
    "s_nop 7\n"
    "s_nop 7\n"
    "s_sub_u32 s82, s82, 1\n"
    "s_cbranch_scc1 Lbcs_30_x\n"
    "buffer_load_dwordx4 v[232:235], v236, s[12:15], 0 offen sc1\n"
    "s_waitcnt vmcnt(0)\n"
    "s_branch Lbcc_29\n"
    "Lbcs_30_x:\n"
    "s_mov_b32 s82, 0\n"
    "s_branch Lbcd_31\n"
    "Lh30s_34:\n"
    "s_nop 7\n"
    "s_nop 7\n"
    "s_sub_u32 s82, s82, 1\n"
    "s_cbranch_scc1 Lh30s_34_x\n"
    "buffer_load_dwordx4 v[160:163], v114, s[12:15], s83 offen offset:0 sc1\n"
    "s_nop 11\n"
    "buffer_load_dwordx4 v[164:167], v114, s[12:15], s83 offen offset:1024 sc1\n"
    "s_nop 11\n"
    "buffer_load_dwordx4 v[168:171], v114, s[12:15], s83 offen offset:2048 sc1\n"
    "s_nop 11\n"
    "buffer_load_dwordx4 v[172:175], v114, s[12:15], s83 offen offset:3072 sc1\n"
    "s_waitcnt vmcnt(0)\n"
    "s_branch Lh30c_33\n"
    "Lh30s_34_x:\n"
    "s_mov_b32 s82, 0\n"
    "s_branch Lh30d_35\n"
    "Lac0s_39:\n"
    "s_nop 7\n"
    "s_nop 7\n"
    "s_sub_u32 s82, s82, 1\n"
    "s_cbranch_scc1 Lac0s_39_x\n"
    "s_sleep 4\n"
    "buffer_load_dwordx4 v[204:207], v208, s[12:15], 0 offen sc1\n"
    "s_waitcnt vmcnt(0)\n"
    "s_branch Lac0c_38\n"
    "Lac0s_39_x:\n"
    "s_mov_b32 s82, 0\n"
    "s_branch Lac0d_40\n"
    "Lqcs_51:\n"
    "s_nop 7\n"
    "s_nop 7\n"
    "s_sub_u32 s82, s82, 1\n"
    "s_cbranch_scc1 Lqcs_51_x\n"
    "buffer_load_dwordx4 v[168:171], v4, s[12:15], 0 offen sc1\n"
    "s_waitcnt vmcnt(0)\n"
    "s_branch Lqcc_50\n"
    "Lqcs_51_x:\n"
    "s_mov_b32 s82, 0\n"
    "s_branch Lqcd_52\n"
    "Lads_48:\n"
    "s_nop 7\n"
    "s_nop 7\n"
    "s_sub_u32 s82, s82, 1\n"
    "s_cbranch_scc1 Lads_48_x\n"
    "s_sleep 8\n"
    "buffer_load_dwordx4 v[204:207], v208, s[12:15], 0 offen sc1\n"
    "s_waitcnt vmcnt(0)\n"
    "s_branch Ladc_47\n"
    "Lads_48_x:\n"
    "s_mov_b32 s82, 0\n"
    "s_branch Ladd_49\n"
    "Lbcs_59:\n"
    "s_nop 7\n"
    "s_nop 7\n"
    "s_sub_u32 s82, s82, 1\n"
    "s_cbranch_scc1 Lbcs_59_x\n"
    "buffer_load_dwordx4 v[232:235], v236, s[12:15], 0 offen sc1\n"
    "s_waitcnt vmcnt(0)\n"
    "s_branch Lbcc_58\n"
    "Lbcs_59_x:\n"
    "s_mov_b32 s82, 0\n"
    "s_branch Lbcd_60\n"
    "Lac0s_63:\n"
    "s_nop 7\n"
    "s_nop 7\n"
    "s_sub_u32 s82, s82, 1\n"
    "s_cbranch_scc1 Lac0s_63_x\n"
    "s_sleep 4\n"
    "buffer_load_dwordx4 v[144:147], v148, s[12:15], 0 offen sc1\n"
    "s_waitcnt vmcnt(0)\n"
    "s_branch Lac0c_62\n"
    "Lac0s_63_x:\n"
    "s_mov_b32 s82, 0\n"
    "s_branch Lac0d_64\n"
    "Lh40s_67:\n"
    "s_nop 7\n"
    "s_nop 7\n"
    "s_sub_u32 s82, s82, 1\n"
    "s_cbranch_scc1 Lh40s_67_x\n"
    "buffer_load_dwordx4 v[144:147], v122, s[12:15], s88 offen sc1\n"
    "s_nop 11\n"
    "buffer_load_dwordx4 v[148:151], v122, s[12:15], s89 offen sc1\n"
    "s_nop 11\n"
    "buffer_load_dwordx4 v[152:155], v122, s[12:15], s90 offen sc1\n"
    "s_nop 11\n"
    "buffer_load_dwordx4 v[156:159], v122, s[12:15], s91 offen sc1\n"
    "s_nop 11\n"
    "buffer_load_dwordx4 v[160:163], v122, s[12:15], s92 offen sc1\n"
    "s_nop 11\n"
    "buffer_load_dwordx4 v[164:167], v122, s[12:15], s93 offen sc1\n"
    "s_nop 11\n"
    "buffer_load_dwordx4 v[168:171], v122, s[12:15], s94 offen sc1\n"
    "s_nop 11\n"
    "buffer_load_dwordx4 v[172:175], v122, s[12:15], s95 offen sc1\n"
    "s_waitcnt vmcnt(0)\n"
    "s_branch Lh40c_66\n"
    "Lh40s_67_x:\n"
    "s_mov_b32 s82, 0\n"
    "s_branch Lh40d_68\n"
    "Lqcs_75:\n"
    "s_nop 7\n"
    "s_nop 7\n"
    "s_sub_u32 s82, s82, 1\n"
    "s_cbranch_scc1 Lqcs_75_x\n"
    "buffer_load_dwordx4 v[168:171], v4, s[12:15], 0 offen sc1\n"
    "s_waitcnt vmcnt(0)\n"
    "s_branch Lqcc_74\n"
    "Lqcs_75_x:\n"
    "s_mov_b32 s82, 0\n"
    "s_branch Lqcd_76\n"
    "Lads_72:\n"
    "s_nop 7\n"
    "s_nop 7\n"
    "s_sub_u32 s82, s82, 1\n"
    "s_cbranch_scc1 Lads_72_x\n"
    "s_sleep 8\n"
    "buffer_load_dwordx4 v[204:207], v208, s[12:15], 0 offen sc1\n"
    "s_waitcnt vmcnt(0)\n"
    "s_branch Ladc_71\n"
    "Lads_72_x:\n"
    "s_mov_b32 s82, 0\n"
    "s_branch Ladd_73\n"
    "Lbcs_83:\n"
    "s_nop 7\n"
    "s_nop 7\n"
    "s_sub_u32 s82, s82, 1\n"
    "s_cbranch_scc1 Lbcs_83_x\n"
    "buffer_load_dwordx4 v[232:235], v236, s[12:15], 0 offen sc1\n"
    "s_waitcnt vmcnt(0)\n"
    "s_branch Lbcc_82\n"
    "Lbcs_83_x:\n"
    "s_mov_b32 s82, 0\n"
    "s_branch Lbcd_84\n"
    "Lac0s_87:\n"
    "s_nop 7\n"
    "s_nop 7\n"
    "s_sub_u32 s82, s82, 1\n"
    "s_cbranch_scc1 Lac0s_87_x\n"
    "s_sleep 4\n"
    "buffer_load_dwordx4 v[152:155], v156, s[12:15], 0 offen sc1\n"
    "s_waitcnt vmcnt(0)\n"
    "s_branch Lac0c_86\n"
    "Lac0s_87_x:\n"
    "s_mov_b32 s82, 0\n"
    "s_branch Lac0d_88\n"
    "Lhr0s_91:\n"
    "s_nop 7\n"
    "s_nop 7\n"
    "s_sub_u32 s82, s82, 1\n"
    "s_cbranch_scc1 Lhr0s_91_x\n"
    "buffer_load_ushort v144, v204, s[12:15], s88 offen sc1\n"
    "buffer_load_ushort v145, v204, s[12:15], s89 offen sc1\n"
    "buffer_load_ushort v146, v204, s[12:15], s90 offen sc1\n"
    "buffer_load_ushort v147, v204, s[12:15], s91 offen sc1\n"
    "buffer_load_ushort v148, v204, s[12:15], s92 offen sc1\n"
    "buffer_load_ushort v149, v204, s[12:15], s93 offen sc1\n"
    "buffer_load_ushort v150, v204, s[12:15], s94 offen sc1\n"
    "buffer_load_ushort v151, v204, s[12:15], s95 offen sc1\n"
    "s_waitcnt vmcnt(0)\n"
    "s_branch Lhr0c_90\n"
    "Lhr0s_91_x:\n"
    "s_mov_b32 s82, 0\n"
    "s_branch Lhr0d_92\n"
    "Lqcs_94:\n"
    "s_nop 7\n"
    "s_nop 7\n"
    "s_sub_u32 s82, s82, 1\n"
    "s_cbranch_scc1 Lqcs_94_x\n"
    "buffer_load_dwordx4 v[164:167], v4, s[12:15], 0 offen sc1\n"
    "s_waitcnt vmcnt(0)\n"
    "s_branch Lqcc_93\n"
    "Lqcs_94_x:\n"
    "s_mov_b32 s82, 0\n"
    "s_branch Lqcd_95\n"
    "Lh50s_98:\n"
    "s_nop 7\n"
    "s_nop 7\n"
    "s_sub_u32 s82, s82, 1\n"
    "s_cbranch_scc1 Lh50s_98_x\n"
    "buffer_load_dwordx4 v[152:155], v200, s[12:15], s83 offen sc1\n"
    "s_nop 11\n"
    "buffer_load_dwordx4 v[156:159], v201, s[12:15], s83 offen sc1\n"
    "s_nop 11\n"
    "buffer_load_dwordx4 v[160:163], v202, s[12:15], s83 offen sc1\n"
    "s_nop 11\n"
    "buffer_load_dwordx4 v[164:167], v203, s[12:15], s83 offen sc1\n"
    "s_waitcnt vmcnt(0)\n"
    "s_branch Lh50c_97\n"
    "Lh50s_98_x:\n"
    "s_mov_b32 s82, 0\n"
    "s_branch Lh50d_99\n"
    "Lads_101:\n"
    "s_nop 7\n"
    "s_nop 7\n"
    "s_sub_u32 s82, s82, 1\n"
    "s_cbranch_scc1 Lads_101_x\n"
    "s_sleep 8\n"
    "buffer_load_dwordx4 v[144:147], v148, s[12:15], 0 offen sc1\n"
    "s_waitcnt vmcnt(0)\n"
    "s_branch Ladc_100\n"
    "Lads_101_x:\n"
    "s_mov_b32 s82, 0\n"
    "s_branch Ladd_102\n"
    "Lhxs_106:\n"
    "s_nop 7\n"
    "s_nop 7\n"
    "s_sub_u32 s82, s82, 1\n"
    "s_cbranch_scc1 Lhxs_106_x\n"
    "buffer_load_dwordx4 v[160:163], v5, s[12:15], 0 offen sc1\n"
    "s_waitcnt vmcnt(0)\n"
    "s_branch Lhxc_105\n"
    "Lhxs_106_x:\n"
    "s_mov_b32 s82, 0\n"
    "s_branch Lhxd_107\n"
    "Lhbs_109:\n"
    "s_nop 7\n"
    "s_nop 7\n"
    "s_sub_u32 s82, s82, 1\n"
    "s_cbranch_scc1 Lhbs_109_x\n"
    "buffer_load_dwordx4 v[12:15], v122, s[12:15], s66 offen sc1\n"
    "s_waitcnt vmcnt(0)\n"
    "s_branch Lhbc_108\n"
    "Lhbs_109_x:\n"
    "s_mov_b32 s82, 0\n"
    "s_branch Lhbd_110\n"
    "Lhq1s_113:\n"
    "s_nop 7\n"
    "s_nop 7\n"
    "s_sub_u32 s82, s82, 1\n"
    "s_cbranch_scc1 Lhq1s_113_x\n"
    "buffer_load_dwordx4 v[184:187], v113, s[12:15], 0 offen offset:0 sc1\n"
    "s_waitcnt vmcnt(0)\n"
    "s_branch Lhq1c_112\n"
    "Lhq1s_113_x:\n"
    "s_mov_b32 s82, 0\n"
    "s_branch Lhq1d_114\n"
    "Lh11s_117:\n"
    "s_nop 7\n"
    "s_nop 7\n"
    "s_sub_u32 s10, s10, 1\n"
    "s_cbranch_scc1 Lh11s_117_x\n"
    "s_waitcnt lgkmcnt(0)\n"
    "s_barrier\n"
    "s_add_u32 m0, s93, 135168\n"
    "s_nop 0\n"
    "buffer_load_dwordx4 v116, s[12:15], s83 offen offset:0 sc1 lds\n"
    "s_add_u32 m0, s93, 135200\n"
    "s_nop 0\n"
    "buffer_load_dwordx4 v116, s[12:15], s83 offen offset:1024 sc1 lds\n"
    "s_add_u32 m0, s94, 135232\n"
    "s_nop 0\n"
    "buffer_load_dwordx4 v116, s[12:15], s83 offen offset:2048 sc1 lds\n"
    "s_add_u32 m0, s94, 135264\n"
    "s_nop 0\n"
    "buffer_load_dwordx4 v116, s[12:15], s83 offen offset:3072 sc1 lds\n"
    "s_waitcnt vmcnt(0)\n"
    "s_branch Lh11c_116\n"
    "Lh11s_117_x:\n"
    "s_mov_b32 s10, 0\n"
    "s_branch Lh11d_118\n"
    "Lh21s_124:\n"
    "s_nop 7\n"
    "s_nop 7\n"
    "s_sub_u32 s10, s10, 1\n"
    "s_cbranch_scc1 Lh21s_124_x\n"
    "s_waitcnt lgkmcnt(0)\n"
    "s_barrier\n"
    "buffer_load_dwordx2 v[144:145], v120, s[12:15], s83 offen sc1\n"
    "s_add_u32 m0, s5, 67584\n"
    "s_nop 0\n"
    "buffer_load_dwordx4 v118, s[12:15], s88 offen offset:0 sc1 lds\n"
    "s_add_u32 m0, s5, 68608\n"
    "s_nop 0\n"
    "buffer_load_dwordx4 v118, s[12:15], s89 offen offset:0 sc1 lds\n"
    "s_sleep 2\n"
    "s_add_u32 m0, s5, 69632\n"
    "s_nop 0\n"
    "buffer_load_dwordx4 v118, s[12:15], s90 offen offset:0 sc1 lds\n"
    "s_add_u32 m0, s5, 70656\n"
    "s_nop 0\n"
    "buffer_load_dwordx4 v118, s[12:15], s91 offen offset:0 sc1 lds\n"
    "s_sleep 2\n"
    "s_add_u32 m0, s5, 71680\n"
    "s_nop 0\n"
    "buffer_load_dwordx4 v118, s[12:15], s92 offen offset:0 sc1 lds\n"
    "s_add_u32 m0, s5, 72704\n"
    "s_nop 0\n"
    "buffer_load_dwordx4 v118, s[12:15], s93 offen offset:0 sc1 lds\n"
    "s_sleep 2\n"
    "s_add_u32 m0, s5, 73728\n"
    "s_nop 0\n"
    "buffer_load_dwordx4 v118, s[12:15], s94 offen offset:0 sc1 lds\n"
    "s_add_u32 m0, s5, 74752\n"
    "s_nop 0\n"
    "buffer_load_dwordx4 v118, s[12:15], s95 offen offset:0 sc1 lds\n"
    "s_waitcnt vmcnt(0)\n"
    "s_branch Lh21c_123\n"
    "Lh21s_124_x:\n"
    "s_mov_b32 s10, 0\n"
    "s_branch Lh21d_125\n"
    "Lh31s_128:\n"
    "s_nop 7\n"
    "s_nop 7\n"
    "s_sub_u32 s82, s82, 1\n"
    "s_cbranch_scc1 Lh31s_128_x\n"
    "buffer_load_dwordx4 v[160:163], v114, s[12:15], s83 offen offset:0 sc1\n"
    "s_nop 11\n"
    "buffer_load_dwordx4 v[164:167], v114, s[12:15], s83 offen offset:1024 sc1\n"
    "s_nop 11\n"
    "buffer_load_dwordx4 v[168:171], v114, s[12:15], s83 offen offset:2048 sc1\n"
    "s_nop 11\n"
    "buffer_load_dwordx4 v[172:175], v114, s[12:15], s83 offen offset:3072 sc1\n"
    "s_waitcnt vmcnt(0)\n"
    "s_branch Lh31c_127\n"
    "Lh31s_128_x:\n"
    "s_mov_b32 s82, 0\n"
    "s_branch Lh31d_129\n"
    "Lh41s_133:\n"
    "s_nop 7\n"
    "s_nop 7\n"
    "s_sub_u32 s82, s82, 1\n"
    "s_cbranch_scc1 Lh41s_133_x\n"
    "buffer_load_dwordx4 v[144:147], v122, s[12:15], s88 offen sc1\n"
    "s_nop 11\n"
    "buffer_load_dwordx4 v[148:151], v122, s[12:15], s89 offen sc1\n"
    "s_nop 11\n"
    "buffer_load_dwordx4 v[152:155], v122, s[12:15], s90 offen sc1\n"
    "s_nop 11\n"
    "buffer_load_dwordx4 v[156:159], v122, s[12:15], s91 offen sc1\n"
    "s_nop 11\n"
    "buffer_load_dwordx4 v[160:163], v122, s[12:15], s92 offen sc1\n"
    "s_nop 11\n"
    "buffer_load_dwordx4 v[164:167], v122, s[12:15], s93 offen sc1\n"
    "s_nop 11\n"
    "buffer_load_dwordx4 v[168:171], v122, s[12:15], s94 offen sc1\n"
    "s_nop 11\n"
    "buffer_load_dwordx4 v[172:175], v122, s[12:15], s95 offen sc1\n"
    "s_waitcnt vmcnt(0)\n"
    "s_branch Lh41c_132\n"
    "Lh41s_133_x:\n"
    "s_mov_b32 s82, 0\n"
    "s_branch Lh41d_134\n"
    "Lhr1s_138:\n"
    "s_nop 7\n"
    "s_nop 7\n"
    "s_sub_u32 s82, s82, 1\n"
    "s_cbranch_scc1 Lhr1s_138_x\n"
    "buffer_load_ushort v144, v204, s[12:15], s88 offen sc1\n"
    "buffer_load_ushort v145, v204, s[12:15], s89 offen sc1\n"
    "buffer_load_ushort v146, v204, s[12:15], s90 offen sc1\n"
    "buffer_load_ushort v147, v204, s[12:15], s91 offen sc1\n"
    "buffer_load_ushort v148, v204, s[12:15], s92 offen sc1\n"
    "buffer_load_ushort v149, v204, s[12:15], s93 offen sc1\n"
    "buffer_load_ushort v150, v204, s[12:15], s94 offen sc1\n"
    "buffer_load_ushort v151, v204, s[12:15], s95 offen sc1\n"
    "s_waitcnt vmcnt(0)\n"
    "s_branch Lhr1c_137\n"
    "Lhr1s_138_x:\n"
    "s_mov_b32 s82, 0\n"
    "s_branch Lhr1d_139\n"
    "Lh51s_142:\n"
    "s_nop 7\n"
    "s_nop 7\n"
    "s_sub_u32 s82, s82, 1\n"
    "s_cbranch_scc1 Lh51s_142_x\n"
    "buffer_load_dwordx4 v[152:155], v200, s[12:15], s83 offen sc1\n"
    "s_nop 11\n"
    "buffer_load_dwordx4 v[156:159], v201, s[12:15], s83 offen sc1\n"
    "s_nop 11\n"
    "buffer_load_dwordx4 v[160:163], v202, s[12:15], s83 offen sc1\n"
    "s_nop 11\n"
    "buffer_load_dwordx4 v[164:167], v203, s[12:15], s83 offen sc1\n"
    "s_waitcnt vmcnt(0)\n"
    "s_branch Lh51c_141\n"
    "Lh51s_142_x:\n"
    "s_mov_b32 s82, 0\n"
    "s_branch Lh51d_143\n"
    "Lend:\n"
    "s_endpgm\n"
    :
    : "s"(__builtin_amdgcn_kernarg_segment_ptr()), "s"(blockIdx.x), "v"(threadIdx.x)
    : "v1", "v2", "v3", "v4", "v5", "v6", "v7", "v8", "v9", "v10", "v11", "v12", "v13", "v14", "v15", "v16", "v17", "v18", "v19", "v20", "v21", "v22", "v23", "v24", "v25", "v26", "v27", "v28", "v29", "v30", "v31", "v32", "v33", "v34", "v35", "v36", "v37", "v38", "v39", "v40", "v41", "v42", "v43", "v44", "v45", "v46", "v47", "v48", "v49", "v50", "v51", "v52", "v53", "v54", "v55", "v56", "v57", "v58", "v59", "v60", "v61", "v62", "v63", "v64", "v65", "v66", "v67", "v68", "v69", "v70", "v71", "v72", "v73", "v74", "v75", "v76", "v77", "v78", "v79", "v80", "v81", "v82", "v83", "v84", "v85", "v86", "v87", "v88", "v89", "v90", "v91", "v92", "v93", "v94", "v95", "v96", "v97", "v98", "v99", "v100", "v101", "v102", "v103", "v104", "v105", "v106", "v107", "v108", "v109", "v110", "v111", "v112", "v113", "v114", "v115", "v116", "v117", "v118", "v119", "v120", "v121", "v122", "v123", "v124", "v125", "v126", "v127", "v128", "v129", "v130", "v131", "v132", "v133", "v134", "v135", "v136", "v137", "v138", "v139", "v140", "v141", "v142", "v143", "v144", "v145", "v146", "v147", "v148", "v149", "v150", "v151", "v152", "v153", "v154", "v155", "v156", "v157", "v158", "v159", "v160", "v161", "v162", "v163", "v164", "v165", "v166", "v167", "v168", "v169", "v170", "v171", "v172", "v173", "v174", "v175", "v176", "v177", "v178", "v179", "v180", "v181", "v182", "v183", "v184", "v185", "v186", "v187", "v188", "v189", "v190", "v191", "v192", "v193", "v194", "v195", "v196", "v197", "v198", "v199", "v200", "v201", "v202", "v203", "v204", "v205", "v206", "v207", "v208", "v209", "v210", "v211", "v212", "v213", "v214", "v215", "v216", "v217", "v218", "v219", "v220", "v221", "v222", "v223", "v224", "v225", "v226", "v227", "v228", "v229", "v230", "v231", "v232", "v233", "v234", "v235", "v236", "v237", "v238", "v239", "v240", "v241", "v242", "v243", "v244", "v245", "v246", "v247", "v248", "v249", "v250", "v251", "v252", "v253", "v254", "v255", "a0", "a1", "a2", "a3", "a4", "a5", "a6", "a7", "a8", "a9", "a10", "a11", "a12", "a13", "a14", "a15", "a16", "a17", "a18", "a19", "a20", "a21", "a22", "a23", "a24", "a25", "a26", "a27", "a28", "a29", "a30", "a31", "a32", "a33", "a34", "a35", "a36", "a37", "a38", "a39", "a40", "a41", "a42", "a43", "a44", "a45", "a46", "a47", "a48", "a49", "a50", "a51", "a52", "a53", "a54", "a55", "a56", "a57", "a58", "a59", "a60", "a61", "a62", "a63", "a64", "a65", "a66", "a67", "a68", "a69", "a70", "a71", "a72", "a73", "a74", "a75", "a76", "a77", "a78", "a79", "a80", "a81", "a82", "a83", "a84", "a85", "a86", "a87", "a88", "a89", "a90", "a91", "a92", "a93", "a94", "a95", "a96", "a97", "a98", "a99", "a100", "a101", "a102", "a103", "a104", "a105", "a106", "a107", "a108", "a109", "a110", "a111", "a112", "a113", "a114", "a115", "a116", "a117", "a118", "a119", "a120", "a121", "a122", "a123", "a124", "a125", "a126", "a127", "a128", "a129", "a130", "a131", "a132", "a133", "a134", "a135", "a136", "a137", "a138", "a139", "a140", "a141", "a142", "a143", "a144", "a145", "a146", "a147", "a148", "a149", "a150", "a151", "a152", "a153", "a154", "a155", "a156", "a157", "a158", "a159", "a160", "a161", "a162", "a163", "a164", "a165", "a166", "a167", "a168", "a169", "a170", "a171", "a172", "a173", "a174", "a175", "a176", "a177", "a178", "a179", "a180", "a181", "a182", "a183", "a184", "a185", "a186", "a187", "a188", "a189", "a190", "a191", "a192", "a193", "a194", "a195", "a196", "a197", "a198", "a199", "a200", "a201", "a202", "a203", "a204", "a205", "a206", "a207", "a208", "a209", "a210", "a211", "a212", "a213", "a214", "a215", "a216", "a217", "a218", "a219", "a220", "a221", "a222", "a223", "a224", "a225", "a226", "a227", "a228", "a229", "a230", "a231", "a232", "a233", "a234", "a235", "a236", "a237", "a238", "a239", "a240", "a241", "a242", "a243", "a244", "a245", "a246", "a247", "a248", "a249", "a250", "a251", "a252", "a253", "a254", "a255", "s3", "s4", "s5", "s6", "s7", "s8", "s9", "s10", "s11", "s12", "s13", "s14", "s15", "s16", "s17", "s18", "s19", "s20", "s21", "s22", "s23", "s24", "s25", "s26", "s27", "s28", "s29", "s30", "s31", "s32", "s33", "s34", "s35", "s36", "s37", "s38", "s39", "s40", "s41", "s42", "s43", "s44", "s45", "s46", "s47", "s48", "s49", "s50", "s51", "s52", "s53", "s54", "s55", "s56", "s57", "s58", "s59", "s60", "s61", "s62", "s63", "s64", "s65", "s66", "s67", "s68", "s69", "s70", "s71", "s72", "s73", "s74", "s75", "s76", "s77", "s78", "s79", "s80", "s81", "s82", "s83", "s84", "s85", "s86", "s87", "s88", "s89", "s90", "s91", "s92", "s93", "s94", "s95", "s96", "s97", "s98", "s99", "vcc", "scc", "memory");
}

extern "C" void kernel_launch(void* const* d_in, const int* in_sizes, int n_in, void* d_out, int out_size, void* d_ws, size_t ws_size,
                              hipStream_t stream) {
  Args a;
  const int order[9] = {2, 0, 3, 1, 4, 5, 7, 8, 9};
  a.b[0].p = d_ws; a.b[0].n = 12u << 20; a.b[0].f = 0x20000u;
  for (int i = 0; i < 9; i++) { a.b[1 + i].p = d_in[order[i]]; a.b[1 + i].n = (unsigned)in_sizes[order[i]] * 2u; a.b[1 + i].f = 0x20000u; }
  a.b[10].p = d_out; a.b[10].n = (unsigned)out_size * 2u; a.b[10].f = 0x20000u;
  (void)hipFuncSetAttribute((const void*)mla_ffn_decode_kernel, hipFuncAttributeMaxDynamicSharedMemorySize, 163840);
  mla_ffn_decode_kernel<<<256, 256, 163840, stream>>>(a);
}
```
